```python
import torch

def _softmax(x, axis=-1):
    return torch.softmax(x, dim=axis)
def _gelu(x):
    return torch.nn.functional.gelu(x, approximate="tanh")
def _silu(x):
    return torch.nn.functional.silu(x)
def _sigmoid(x):
    return torch.sigmoid(x)
def _layernorm(x, axis=-1, eps=1e-5):
    m = x.mean(dim=axis, keepdim=True)
    v = x.var(dim=axis, keepdim=True, unbiased=False)
    return (x - m) / torch.sqrt(v + eps)
def _scatter_add(tgt, idx, src, axis=0):
    return tgt.clone().index_add_(axis, idx, src)
def _segment_sum(src, seg, n):
    out = torch.zeros((n,) + src.shape[1:], dtype=src.dtype)
    return out.index_add_(0, seg, src)
def _pad(x, pw):
    flat = tuple(v for pair in reversed(pw) for v in pair)
    return torch.nn.functional.pad(x, flat)

kernel_name = 'sequential_preLN__grouped_kv_mqa__mixer_token_mlp__prompt'

def setup_inputs():

    _w = (lambda a: (lambda t: t.to(torch.bfloat16) if t.is_floating_point() else t)(torch.as_tensor(a)))
    def _A_setup():
        B, S, D, H = 1, 2048, 448, 16
        Dh = D // H
        return {
            "x": _w((lambda *s: torch.randn(*s, dtype=torch.bfloat16) * 0.05)(B, S, D)),
            "wq": _w((lambda *s: torch.randn(*s, dtype=torch.bfloat16) * 0.05)(D, H * Dh)),
            "wk": _w((lambda *s: torch.randn(*s, dtype=torch.bfloat16) * 0.05)(D, Dh)),
            "wv": _w((lambda *s: torch.randn(*s, dtype=torch.bfloat16) * 0.05)(D, Dh)),
            "wo": _w((lambda *s: torch.randn(*s, dtype=torch.bfloat16) * 0.05)(H * Dh, D)),
        }
    _A_in = _A_setup()

    def _B_setup():
        B, S, D, F = 1, 2048, 448, 1792
        return {
            "x": _w((lambda *s: torch.randn(*s, dtype=torch.bfloat16) * 0.05)(B, S, D)),
            "w1": _w((lambda *s: torch.randn(*s, dtype=torch.bfloat16) * 0.8)(S, F) * (S ** -0.5)),
            "b1": _w((lambda *s: torch.randn(*s, dtype=torch.bfloat16) * 12.8)(F) * 0.02),
            "w2": _w((lambda *s: torch.randn(*s, dtype=torch.bfloat16) * 0.2)(F, S) * (F ** -0.5)),
            "b2": _w((lambda *s: torch.randn(*s, dtype=torch.bfloat16) * 3.2)(S) * 0.02),
        }
    _B_in = _B_setup()

    return {
        "A_x": _A_in["x"],
        "A_wq": _A_in["wq"],
        "A_wk": _A_in["wk"],
        "A_wv": _A_in["wv"],
        "A_wo": _A_in["wo"],
        "B_x": _B_in["x"],
        "B_w1": _B_in["w1"],
        "B_b1": _B_in["b1"],
        "B_w2": _B_in["w2"],
        "B_b2": _B_in["b2"],
    }

def reference(A_x, A_wq, A_wk, A_wv, A_wo, B_x, B_w1, B_b1, B_w2, B_b2):
    def _A_ref(x, wq, wk, wv, wo):
        B, S, D = x.shape
        H = 16
        Dh = D // H
        q = (lambda x, *d: x.permute(*d))((x @ wq).reshape(B, S, H, Dh), 0, 2, 1, 3)
        k = x @ wk
        v = x @ wv
        scores = torch.einsum("bhqd,bkd->bhqk", q, k) * (Dh ** -0.5)
        p = _softmax(scores, axis=-1)
        o = torch.einsum("bhqk,bkd->bhqd", p, v)
        return (lambda x, *d: x.permute(*d))(o, 0, 2, 1, 3).reshape(B, S, H * Dh) @ wo

    def _B_ref(x, w1, b1, w2, b2):
        B, S, D = x.shape
        h = _layernorm(x, axis=-1)
        u = torch.einsum("bsd,sf->bdf", h, w1) + b1
        u = _gelu(u)
        y = torch.einsum("bdf,fs->bds", u, w2) + b2
        return (lambda x, *d: x.permute(*d))(y, 0, 2, 1)

    x = A_x
    h = x + _A_ref(_layernorm(x, axis=-1), A_wq, A_wk, A_wv, A_wo)
    h2 = h + _B_ref(_layernorm(h, axis=-1), B_w1, B_b1, B_w2, B_b2)
    return h2
```

```cpp
#include <hip/hip_runtime.h>
#include <hip/hip_bf16.h>
#include <stdint.h>

typedef unsigned short u16;
typedef __attribute__((ext_vector_type(8))) short bf16x8;
typedef __attribute__((ext_vector_type(4))) short s16x4;
typedef __attribute__((ext_vector_type(4))) float f32x4;
typedef __attribute__((ext_vector_type(16))) float f32x16;
typedef __attribute__((ext_vector_type(4))) unsigned u32x4;
typedef __attribute__((ext_vector_type(2))) unsigned u32x2;

#define DI __device__ __forceinline__
#define MFMA32(a, b, c) __builtin_amdgcn_mfma_f32_32x32x16_bf16((a), (b), (c), 0, 0, 0)
#define MFMA16(a, b, c) __builtin_amdgcn_mfma_f32_16x16x32_bf16((a), (b), (c), 0, 0, 0)

constexpr int S_ = 2048, D_ = 448, H_ = 16, DH_ = 28, F_ = 1792;

constexpr size_t OFF_Q   = 0;
constexpr size_t OFF_KF  = OFF_Q + (size_t)S_ * H_ * 32 * 2;
constexpr size_t OFF_VF  = OFF_KF + (size_t)S_ * 32 * 2;
constexpr size_t OFF_WOF = OFF_VF + (size_t)S_ * 32 * 2;
constexpr size_t OFF_H   = OFF_WOF + (size_t)D_ * D_ * 2;
constexpr size_t OFF_HNF = OFF_H + (size_t)S_ * D_ * 2;
constexpr size_t OFF_UF  = OFF_HNF + (size_t)S_ * D_ * 2;

DI float bf2f(unsigned v) { return __uint_as_float(v << 16); }
DI float bflo(unsigned v) { return __uint_as_float(v << 16); }
DI float bfhi(unsigned v) { return __uint_as_float(v & 0xffff0000u); }
DI unsigned pack2(float a, float b) {
  typedef __attribute__((ext_vector_type(2))) __bf16 bf2;
  typedef __attribute__((ext_vector_type(2))) float f2;
  f2 v = {a, b};
  bf2 r = __builtin_convertvector(v, bf2);
  return __builtin_bit_cast(unsigned, r);
}
DI float bfr(float a) { return bflo(pack2(a, 0.f)); }
DI bf16x8 asbf(u32x4 v) { return __builtin_bit_cast(bf16x8, v); }

DI void glds16(const void* g, void* l) {
  __builtin_amdgcn_global_load_lds((const unsigned*)g, (unsigned*)l, 16, 0, 0);
}
DI s16x4 tr_read(const char* p) {
  return __builtin_amdgcn_ds_read_tr16_b64_v4i16((__attribute__((address_space(3))) s16x4*)(p));
}
DI bf16x8 cat(s16x4 lo, s16x4 hi) { return __builtin_shufflevector(lo, hi, 0, 1, 2, 3, 4, 5, 6, 7); }

__global__ __launch_bounds__(256) void k1_ln_qkv(const u16* __restrict__ x, const u16* __restrict__ wq,
                                                 const u16* __restrict__ wk, const u16* __restrict__ wv,
                                                 const u16* __restrict__ wo, u16* __restrict__ Q,
                                                 u16* __restrict__ Kf, u16* __restrict__ Vf,
                                                 u16* __restrict__ WoF) {
  __shared__ __attribute__((aligned(16))) char smem[D_ * 128];
  const int tid = threadIdx.x, l = tid & 63;
  const int w = __builtin_amdgcn_readfirstlane(tid >> 6);
  const int bx = blockIdx.x, xcd = bx & 7, ii = bx >> 3;
  const int tb = xcd + 8 * (ii >> 3), nt = ii & 7;
  const int g = l >> 4, i16 = l & 15;

  if (nt < 7) {
#pragma unroll
    for (int it = 0; it < 14; ++it) {
      const int piece = it * 4 + w;
      const int r = piece * 8 + (l >> 3);
      const int fr = ((r >> 1) & 1) | (((r >> 3) & 1) << 1);
      const int c = ((l & 7) >> 1) ^ fr;
      glds16(wq + r * D_ + nt * 64 + (2 * c + (l & 1)) * 8, smem + piece * 1024);
    }
  } else {
    for (int idx = tid; idx < D_ * 32; idx += 256) {
      const int r = idx >> 5, col = (idx & 31) * 2;
      unsigned v = 0;
      if (col < DH_) v = *(const unsigned*)(wk + r * DH_ + col);
      else if (col >= 32 && col < 32 + DH_) v = *(const unsigned*)(wv + r * DH_ + col - 32);
      const int fr = ((r >> 1) & 1) | (((r >> 3) & 1) << 1);
      *(unsigned*)(smem + r * 128 + (((col >> 4) ^ fr) * 32) + (col & 15) * 2) = v;
    }
  }

  if (w < 2 && bx < 196) {
    const int fi = 2 * bx + w, t = fi / 14, s = fi % 14;
    const u16* src = wo + (32 * s + 8 * g) * D_ + 16 * t + i16;
    u32x4 v;
#pragma unroll
    for (int j = 0; j < 4; ++j) v[j] = (unsigned)src[(2 * j) * D_] | ((unsigned)src[(2 * j + 1) * D_] << 16);
    *(u32x4*)(WoF + ((size_t)fi * 64 + l) * 8) = v;
  }

  const int tok = tb * 64 + w * 16 + i16;
  u32x4 xf[14];
#pragma unroll
  for (int s = 0; s < 14; ++s) xf[s] = *(const u32x4*)(x + tok * D_ + 32 * s + 8 * g);
  float sum = 0.f;
#pragma unroll
  for (int s = 0; s < 14; ++s)
#pragma unroll
    for (int j = 0; j < 4; ++j) sum += bflo(xf[s][j]) + bfhi(xf[s][j]);
  sum += __shfl_xor(sum, 16);
  sum += __shfl_xor(sum, 32);
  const float mean = sum * (1.0f / D_);
  float sq = 0.f;
#pragma unroll
  for (int s = 0; s < 14; ++s)
#pragma unroll
    for (int j = 0; j < 4; ++j) {
      const float a = bflo(xf[s][j]) - mean, b = bfhi(xf[s][j]) - mean;
      sq += a * a + b * b;
    }
  sq += __shfl_xor(sq, 16);
  sq += __shfl_xor(sq, 32);
  {
    const float mb = bfr(mean);
    const float vb = bfr(sq * (1.0f / D_));
    const float sd = bfr(__builtin_sqrtf(bfr(vb + 1e-5f)));
    const float rs = 1.0f / sd;
#pragma unroll
    for (int s = 0; s < 14; ++s)
#pragma unroll
      for (int j = 0; j < 4; ++j) {
        const unsigned dd = pack2(bflo(xf[s][j]) - mb, bfhi(xf[s][j]) - mb);
        xf[s][j] = pack2(bflo(dd) * rs, bfhi(dd) * rs);
      }
  }

  asm volatile("s_waitcnt vmcnt(0)" ::: "memory");
  __syncthreads();

  const int q4 = i16 >> 2, p4 = l & 3;
  const int fl = ((q4 >> 1) & 1) | ((g & 1) << 1);
  const char* tb0 = smem + (8 * g + q4) * 128 + p4 * 8;
  f32x4 acc[4];
#pragma unroll
  for (int t = 0; t < 4; ++t) acc[t] = (f32x4){0.f, 0.f, 0.f, 0.f};
  if (nt < 7) {
#pragma unroll
    for (int s = 0; s < 14; ++s)
#pragma unroll
      for (int t = 0; t < 4; ++t) {
        const char* p = tb0 + s * 4096 + ((t ^ fl) * 32);
        const bf16x8 wf = cat(tr_read(p), tr_read(p + 512));
        acc[t] = MFMA16(wf, asbf(xf[s]), acc[t]);
      }
    const float qs = 0.18898223650461362f * 1.4426950408889634f;
#pragma unroll
    for (int t = 0; t < 4; ++t) {
      const int n = nt * 64 + 16 * t + 4 * g;
      const int head = n / DH_, d = n - head * DH_;
      u32x2 o;
      o[0] = pack2(acc[t][0] * qs, acc[t][1] * qs);
      o[1] = pack2(acc[t][2] * qs, acc[t][3] * qs);
      *(u32x2*)(Q + ((size_t)tok * H_ + head) * 32 + d) = o;
    }
  } else {
#pragma unroll
    for (int s = 0; s < 14; ++s)
#pragma unroll
      for (int t = 0; t < 4; ++t) {
        const char* p = tb0 + s * 4096 + ((t ^ fl) * 32);
        const bf16x8 wf = cat(tr_read(p), tr_read(p + 512));
        if (t < 2) acc[t] = MFMA16(wf, asbf(xf[s]), acc[t]);
        else       acc[t] = MFMA16(asbf(xf[s]), wf, acc[t]);
      }
#pragma unroll
    for (int t = 0; t < 2; ++t) {
      const int d = 16 * t + 4 * g;
      u32x2 o;
      o[0] = pack2(acc[t][0], acc[t][1]);
      o[1] = pack2(acc[t][2], acc[t][3]);
      if (d == 28) { o[0] = 0x3F803F80u; o[1] = 0x00003F80u; }
      const int blk = tok >> 5, hh = (d & 15) >> 3, j = d & 7;
      *(u32x2*)(Kf + ((size_t)((blk * 2 + t) * 64 + (tok & 31) + 32 * hh)) * 8 + j) = o;
    }
#pragma unroll
    for (int t = 2; t < 4; ++t) {
      const int d = 16 * (t - 2) + i16;
      u32x2 o;
      o[0] = pack2(acc[t][0], acc[t][1]);
      o[1] = pack2(acc[t][2], acc[t][3]);
      if (d == 28) { o[0] = 0x3F803F80u; o[1] = 0x3F803F80u; }
      const int blk = tb * 2 + (w >> 1), s = w & 1;
      *(u32x2*)(Vf + ((size_t)((blk * 2 + s) * 64 + d + 32 * (g & 1))) * 8 + 4 * (g >> 1)) = o;
    }
  }
}

constexpr int PART_ROW = 144;
constexpr int PART_W = 128 * PART_ROW;
constexpr int K2_OBUF = 8 * PART_W;
constexpr int K2_SMEM = K2_OBUF + 8 * D_ * 2;

DI float wave_sum(float v) {
#pragma unroll
  for (int o = 32; o > 0; o >>= 1) v += __shfl_xor(v, o);
  return v;
}

__global__ __launch_bounds__(512, 2) void k2_attn(const u16* __restrict__ Q, const u16* __restrict__ Kf,
                                                  const u16* __restrict__ Vf, const u16* __restrict__ WoF,
                                                  const u16* __restrict__ x, u16* __restrict__ Hout,
                                                  u16* __restrict__ HnF) {
  extern __shared__ __attribute__((aligned(16))) char smem[];
  const int tid = threadIdx.x, l = tid & 63, h = l >> 5;
  const int w = __builtin_amdgcn_readfirstlane(tid >> 6);
  const int t0 = blockIdx.x * 8;

  u32x4 qf[4][2];
#pragma unroll
  for (int b = 0; b < 4; ++b)
#pragma unroll
    for (int s = 0; s < 2; ++s)
      qf[b][s] = *(const u32x4*)(Q + ((size_t)(t0 * H_ + 32 * b + (l & 31))) * 32 + 16 * s + 8 * h);
  const u32x4* kbase = (const u32x4*)Kf + (size_t)w * 8 * 128 + l;
  const u32x4* vbase = (const u32x4*)Vf + (size_t)w * 8 * 128 + l;

  f32x16 zero16;
#pragma unroll
  for (int i = 0; i < 16; ++i) zero16[i] = 0.f;

  float mval[4];
  f32x16 o[4];
  int nmax = 1;
  for (;;) {
    float mx[4];
#pragma unroll
    for (int b = 0; b < 4; ++b) {
      mx[b] = -3.0e38f;
      if (h) { qf[b][1][2] = 0u; qf[b][1][3] = 0u; }
    }
    for (int kb = 0; kb < nmax; ++kb) {
      const u32x4 k0 = kbase[kb * 128], k1 = kbase[kb * 128 + 64];
#pragma unroll
      for (int b = 0; b < 4; ++b) {
        f32x16 s = MFMA32(asbf(k0), asbf(qf[b][0]), zero16);
        s = MFMA32(asbf(k1), asbf(qf[b][1]), s);
#pragma unroll
        for (int i = 0; i < 16; ++i) mx[b] = fmaxf(mx[b], s[i]);
      }
    }
#pragma unroll
    for (int b = 0; b < 4; ++b) {
      const float m = fmaxf(mx[b], __shfl_xor(mx[b], 32));
      mval[b] = m;
      const float nm = -m;
      const unsigned hi = __float_as_uint(nm) & 0xffff0000u;
      const float r1 = nm - __uint_as_float(hi);
      const unsigned mid = __float_as_uint(r1) & 0xffff0000u;
      const float r2 = r1 - __uint_as_float(mid);
      const unsigned lo = __float_as_uint(r2) & 0xffff0000u;
      if (h) { qf[b][1][2] = (hi >> 16) | mid; qf[b][1][3] = lo >> 16; }
      o[b] = zero16;
    }
    u32x4 k0n = kbase[0], k1n = kbase[64], v0n = vbase[0], v1n = vbase[64];
#pragma unroll 1
    for (int kb = 0; kb < 8; ++kb) {
      const u32x4 k0 = k0n, k1 = k1n, v0 = v0n, v1 = v1n;
      const int kn = (kb + 1) & 7;
      k0n = kbase[kn * 128]; k1n = kbase[kn * 128 + 64];
      v0n = vbase[kn * 128]; v1n = vbase[kn * 128 + 64];
#pragma unroll
      for (int b = 0; b < 4; ++b) {
        f32x16 s = MFMA32(asbf(k0), asbf(qf[b][0]), zero16);
        s = MFMA32(asbf(k1), asbf(qf[b][1]), s);
        u32x4 p0, p1;
#pragma unroll
        for (int i = 0; i < 4; ++i) {
          p0[i] = pack2(__builtin_amdgcn_exp2f(s[2 * i]), __builtin_amdgcn_exp2f(s[2 * i + 1]));
          p1[i] = pack2(__builtin_amdgcn_exp2f(s[8 + 2 * i]), __builtin_amdgcn_exp2f(s[8 + 2 * i + 1]));
        }
        o[b] = MFMA32(asbf(v0), asbf(p0), o[b]);
        o[b] = MFMA32(asbf(v1), asbf(p1), o[b]);
      }
    }
    bool ok = true;
#pragma unroll
    for (int b = 0; b < 4; ++b) ok = ok && (h == 0 || o[b][12] < 1.8446744e19f);
    if (nmax == 8 || __all(ok)) break;
    nmax = 8;
  }

  {
    char* pw = smem + w * PART_W;
#pragma unroll
    for (int b = 0; b < 4; ++b) {
      char* pr = pw + (32 * b + (l & 31)) * PART_ROW + 16 * h;
#pragma unroll
      for (int gq = 0; gq < 4; ++gq)
        *(f32x4*)(pr + 32 * gq) = (f32x4){o[b][4 * gq], o[b][4 * gq + 1], o[b][4 * gq + 2], o[b][4 * gq + 3]};
      if (h == 0) *(float*)(pr + 128) = mval[b];
    }
  }
  __syncthreads();
  {
    const int row = tid >> 2, qd = tid & 3;
    float mw[8], M = -3.0e38f;
#pragma unroll
    for (int ww = 0; ww < 8; ++ww) {
      mw[ww] = *(const float*)(smem + ww * PART_W + row * PART_ROW + 128);
      M = fmaxf(M, mw[ww]);
    }
    float lsum = 0.f;
    f32x4 a0 = {0.f, 0.f, 0.f, 0.f}, a1 = {0.f, 0.f, 0.f, 0.f};
#pragma unroll
    for (int ww = 0; ww < 8; ++ww) {
      const float e = __builtin_amdgcn_exp2f(mw[ww] - M);
      const char* pr = smem + ww * PART_W + row * PART_ROW;
      lsum += e * *(const float*)(pr + 112);
      a0 += e * *(const f32x4*)(pr + 32 * qd);
      a1 += e * *(const f32x4*)(pr + 32 * qd + 16);
    }
    const float inv = 1.0f / lsum;
    char* ob = smem + K2_OBUF + (row >> 4) * (D_ * 2) + (row & 15) * (DH_ * 2) + 16 * qd;
    u32x2 o0, o1;
    o0[0] = pack2(a0[0] * inv, a0[1] * inv); o0[1] = pack2(a0[2] * inv, a0[3] * inv);
    o1[0] = pack2(a1[0] * inv, a1[1] * inv); o1[1] = pack2(a1[2] * inv, a1[3] * inv);
    *(u32x2*)ob = o0;
    if (qd < 3) *(u32x2*)(ob + 8) = o1;
  }
  __syncthreads();
  {
    const int kh = w >> 2, nq = w & 3, g = l >> 4;
    f32x4 acc[7];
#pragma unroll
    for (int i = 0; i < 7; ++i) acc[i] = (f32x4){0.f, 0.f, 0.f, 0.f};
    const u32x4* wbase = (const u32x4*)WoF + (size_t)((7 * nq) * 14 + 7 * kh) * 64 + l;
#pragma unroll
    for (int s = 0; s < 7; ++s) {
      const u32x4 of = *(const u32x4*)(smem + K2_OBUF + (l & 7) * (D_ * 2) + 64 * (7 * kh + s) + 16 * g);
#pragma unroll
      for (int i = 0; i < 7; ++i) acc[i] = MFMA16(asbf(wbase[(i * 14 + s) * 64]), asbf(of), acc[i]);
    }
    if ((l & 15) < 8) {
#pragma unroll
      for (int i = 0; i < 7; ++i)
        *(f32x4*)(smem + ((kh * 8 + (l & 15)) * D_ + 16 * (7 * nq + i) + 4 * g) * 4) = acc[i];
    }
  }
  __syncthreads();
  {
    const float* r0 = (const float*)smem + w * D_;
    const float* r1 = (const float*)smem + (8 + w) * D_;
    const int tok = t0 + w;
    float v[7];
    float sum = 0.f;
#pragma unroll
    for (int i = 0; i < 7; ++i) {
      const int n = 64 * i + l;
      const float a = bfr(r0[n] + r1[n]);
      v[i] = bfr(bf2f(x[tok * D_ + n]) + a);
      Hout[tok * D_ + n] = (u16)(__float_as_uint(v[i]) >> 16);
      sum += v[i];
    }
#pragma unroll
    for (int rep = 0; rep < 2; ++rep) {
      if (rep) {
        sum = 0.f;
#pragma unroll
        for (int i = 0; i < 7; ++i) sum += v[i];
      }
      const float mean = wave_sum(sum) * (1.0f / D_);
      float sq = 0.f;
#pragma unroll
      for (int i = 0; i < 7; ++i) { const float a = v[i] - mean; sq += a * a; }
      const float var = wave_sum(sq) * (1.0f / D_);
      const float mb = bfr(mean);
      const float sd = bfr(__builtin_sqrtf(bfr(bfr(var) + 1e-5f)));
      const float rs = 1.0f / sd;
#pragma unroll
      for (int i = 0; i < 7; ++i) v[i] = bfr(bfr(v[i] - mb) * rs);
    }
    u16* hb = (u16*)(smem + K2_OBUF) + w * D_;
#pragma unroll
    for (int i = 0; i < 7; ++i) hb[64 * i + l] = (u16)(__float_as_uint(v[i]) >> 16);
  }
  __syncthreads();
  if (tid < D_) {
    const u16* hb = (const u16*)(smem + K2_OBUF);
    u32x4 v;
#pragma unroll
    for (int j = 0; j < 4; ++j)
      v[j] = (unsigned)hb[(2 * j) * D_ + tid] | ((unsigned)hb[(2 * j + 1) * D_ + tid] << 16);
    const int lane = ((t0 >> 3) & 3) * 16 + (tid & 15);
    *(u32x4*)(HnF + ((size_t)(((tid >> 4) * 64 + (t0 >> 5)) * 64 + lane)) * 8) = v;
  }
}

constexpr int G_SMEM = 4 * 4 * 8192;
constexpr int RED_ROW = 272;

template <int N> DI void wait_vm() { asm volatile("s_waitcnt vmcnt(%0)" ::"i"(N) : "memory"); }

template <int MODE>
__global__ __launch_bounds__(256, 1) void k_gemm(const u16* __restrict__ W, const u16* __restrict__ act,
                                                 const u16* __restrict__ bias, const u16* __restrict__ Hres,
                                                 u16* __restrict__ out) {
  constexpr int LDW = MODE == 0 ? F_ : S_;
  constexpr int NKS = MODE == 0 ? S_ / 32 : F_ / 32;
  constexpr int KPW = NKS / 4;
  extern __shared__ __attribute__((aligned(16))) char smem[];
  const int tid = threadIdx.x, l = tid & 63;
  const int w = __builtin_amdgcn_readfirstlane(tid >> 6);
  int dt, nt;
  {
    const int xcd = blockIdx.x & 7, i = blockIdx.x >> 3;
    int j;
    if (MODE == 0) {
      const int start = (xcd * 49) >> 1, cnt = ((xcd + 1) * 49 >> 1) - start;
      if (i >= cnt) return;
      j = start + i;
    } else {
      j = xcd * 28 + i;
    }
    nt = j / 7; dt = j - nt * 7;
  }
  char* ring = smem + w * 32768;
  const int g = l >> 4, i16 = l & 15, q4 = i16 >> 2, p4 = l & 3;
  const u16* asrc = act + ((size_t)(4 * dt) * NKS + w * KPW) * 512 + l * 8;
  const int fsrc = (l >> 4) & 1;
  const u16* wsrc = W + (size_t)(w * KPW * 32 + (l >> 3)) * LDW + 64 * nt + (l & 1) * 8;
  const int c0 = (l & 7) >> 1;

  auto issue = [&](int i) {
    char* slot = ring + (i & 3) * 8192;
#pragma unroll
    for (int j = 0; j < 4; ++j) glds16(asrc + ((size_t)j * NKS + i) * 512, slot + j * 1024);
#pragma unroll
    for (int j = 0; j < 4; ++j) {
      const int c = c0 ^ (fsrc | ((j & 1) << 1));
      glds16(wsrc + (size_t)(i * 32 + 8 * j) * LDW + c * 16, slot + 4096 + j * 1024);
    }
  };

  f32x4 acc[4][4];
#pragma unroll
  for (int a = 0; a < 4; ++a)
#pragma unroll
    for (int b = 0; b < 4; ++b) acc[a][b] = (f32x4){0.f, 0.f, 0.f, 0.f};

  const int fl = ((q4 >> 1) & 1) | ((g & 1) << 1);
  const char* trb = ring + 4096 + (8 * g + q4) * 128 + p4 * 8;
  const char* arb = ring + l * 16;

#pragma unroll
  for (int i = 0; i < 4; ++i) issue(i);

#pragma unroll
  for (int i = 0; i < KPW; ++i) {
    if (i + 3 < KPW) wait_vm<24>();
    else if (i + 2 < KPW) wait_vm<16>();
    else if (i + 1 < KPW) wait_vm<8>();
    else wait_vm<0>();
    const int so = (i & 3) * 8192;
    bf16x8 af[4], wf[4];
#pragma unroll
    for (int j = 0; j < 4; ++j) af[j] = *(const bf16x8*)(arb + so + j * 1024);
#pragma unroll
    for (int t = 0; t < 4; ++t) {
      const char* p = trb + so + ((t ^ fl) * 32);
      wf[t] = cat(tr_read(p), tr_read(p + 512));
    }
    asm volatile("s_waitcnt lgkmcnt(0)" ::: "memory");
    __builtin_amdgcn_sched_barrier(0);
    if (i + 4 < KPW) issue(i + 4);
#pragma unroll
    for (int a = 0; a < 4; ++a)
#pragma unroll
      for (int b = 0; b < 4; ++b) {
        if (MODE == 0) acc[a][b] = MFMA16(wf[a], af[b], acc[a][b]);
        else           acc[a][b] = MFMA16(af[a], wf[b], acc[a][b]);
      }
  }
  __syncthreads();
  {
    char* rw = smem + w * (64 * RED_ROW);
#pragma unroll
    for (int a = 0; a < 4; ++a)
#pragma unroll
      for (int b = 0; b < 4; ++b)
        *(f32x4*)(rw + (16 * b + i16) * RED_ROW + (16 * a + 4 * g) * 4) = acc[a][b];
  }
  __syncthreads();
#pragma unroll
  for (int rep = 0; rep < 2; ++rep) {
    const int idx = rep * 256 + tid;
    const int col = idx >> 3, r8 = (idx & 7) * 8;
    f32x4 s0 = {0.f, 0.f, 0.f, 0.f}, s1 = {0.f, 0.f, 0.f, 0.f};
#pragma unroll
    for (int ww = 0; ww < 4; ++ww) {
      const char* p = smem + ww * (64 * RED_ROW) + col * RED_ROW + r8 * 4;
      s0 += *(const f32x4*)p;
      s1 += *(const f32x4*)(p + 16);
    }
    float v[8] = {s0[0], s0[1], s0[2], s0[3], s1[0], s1[1], s1[2], s1[3]};
    u32x4 ov;
    if (MODE == 0) {
      const int d = 64 * dt + col, f = 64 * nt + r8;
      const u32x4 bv = *(const u32x4*)(bias + f);
#pragma unroll
      for (int j = 0; j < 8; ++j) {
        const float bb = (j & 1) ? bfhi(bv[j >> 1]) : bflo(bv[j >> 1]);
        const float t = bfr(bfr(v[j]) + bb);
        const float y = 0.7978845608028654f * (t + 0.044715f * t * t * t);
        v[j] = t / (1.0f + __expf(-2.0f * y));
      }
#pragma unroll
      for (int j = 0; j < 4; ++j) ov[j] = pack2(v[2 * j], v[2 * j + 1]);
      const int lane = ((f & 31) >> 3) * 16 + (d & 15);
      *(u32x4*)(out + ((size_t)((d >> 4) * (F_ / 32) + (f >> 5)) * 64 + lane) * 8) = ov;
    } else {
      const int s = 64 * nt + col, d = 64 * dt + r8;
      const float bb = bf2f(bias[s]);
      const u32x4 hv = *(const u32x4*)(Hres + (size_t)s * D_ + d);
#pragma unroll
      for (int j = 0; j < 8; ++j) {
        const float hh = (j & 1) ? bfhi(hv[j >> 1]) : bflo(hv[j >> 1]);
        v[j] = hh + bfr(bfr(v[j]) + bb);
      }
#pragma unroll
      for (int j = 0; j < 4; ++j) ov[j] = pack2(v[2 * j], v[2 * j + 1]);
      *(u32x4*)(out + (size_t)s * D_ + d) = ov;
    }
  }
}

extern "C" void kernel_launch(void* const* d_in, const int* in_sizes, int n_in, void* d_out, int out_size,
                              void* d_ws, size_t ws_size, hipStream_t stream) {
  const u16* x  = (const u16*)d_in[0];
  const u16* wq = (const u16*)d_in[1];
  const u16* wk = (const u16*)d_in[2];
  const u16* wv = (const u16*)d_in[3];
  const u16* wo = (const u16*)d_in[4];
  const u16* w1 = (const u16*)d_in[6];
  const u16* b1 = (const u16*)d_in[7];
  const u16* w2 = (const u16*)d_in[8];
  const u16* b2 = (const u16*)d_in[9];
  char* ws = (char*)d_ws;
  u16* Q   = (u16*)(ws + OFF_Q);
  u16* Kf  = (u16*)(ws + OFF_KF);
  u16* Vf  = (u16*)(ws + OFF_VF);
  u16* WoF = (u16*)(ws + OFF_WOF);
  u16* Hb  = (u16*)(ws + OFF_H);
  u16* HnF = (u16*)(ws + OFF_HNF);
  u16* UF  = (u16*)(ws + OFF_UF);

  hipFuncSetAttribute((const void*)k2_attn, hipFuncAttributeMaxDynamicSharedMemorySize, K2_SMEM);
  hipFuncSetAttribute((const void*)k_gemm<0>, hipFuncAttributeMaxDynamicSharedMemorySize, G_SMEM);
  hipFuncSetAttribute((const void*)k_gemm<1>, hipFuncAttributeMaxDynamicSharedMemorySize, G_SMEM);

  k1_ln_qkv<<<256, 256, 0, stream>>>(x, wq, wk, wv, wo, Q, Kf, Vf, WoF);
  k2_attn<<<256, 512, K2_SMEM, stream>>>(Q, Kf, Vf, WoF, x, Hb, HnF);
  k_gemm<0><<<8 * 25, 256, G_SMEM, stream>>>(w1, HnF, b1, nullptr, UF);
  k_gemm<1><<<8 * 28, 256, G_SMEM, stream>>>(w2, UF, b2, Hb, (u16*)d_out);
}
```

```cpp
#include <hip/hip_runtime.h>
#include <hip/hip_bf16.h>
#include <stdint.h>

typedef unsigned short u16;
typedef __attribute__((ext_vector_type(8))) short bf16x8;
typedef __attribute__((ext_vector_type(4))) short s16x4;
typedef __attribute__((ext_vector_type(4))) float f32x4;
typedef __attribute__((ext_vector_type(16))) float f32x16;
typedef __attribute__((ext_vector_type(4))) unsigned u32x4;
typedef __attribute__((ext_vector_type(2))) unsigned u32x2;

#define DI __device__ __forceinline__
#define MFMA32(a, b, c) __builtin_amdgcn_mfma_f32_32x32x16_bf16((a), (b), (c), 0, 0, 0)
#define MFMA16(a, b, c) __builtin_amdgcn_mfma_f32_16x16x32_bf16((a), (b), (c), 0, 0, 0)

constexpr int S_ = 2048, D_ = 448, H_ = 16, DH_ = 28, F_ = 1792;

constexpr size_t OFF_Q   = 0;
constexpr size_t OFF_KF  = OFF_Q + (size_t)S_ * H_ * 32 * 2;
constexpr size_t OFF_VF  = OFF_KF + (size_t)S_ * 32 * 2;
constexpr size_t OFF_WOF = OFF_VF + (size_t)S_ * 32 * 2;
constexpr size_t OFF_H   = OFF_WOF + (size_t)D_ * D_ * 2;
constexpr size_t OFF_HNF = OFF_H + (size_t)S_ * D_ * 2;
constexpr size_t OFF_UF  = OFF_HNF + (size_t)S_ * D_ * 2;

DI float bf2f(unsigned v) { return __uint_as_float(v << 16); }
DI float bflo(unsigned v) { return __uint_as_float(v << 16); }
DI float bfhi(unsigned v) { return __uint_as_float(v & 0xffff0000u); }
DI unsigned pack2(float a, float b) {
  typedef __attribute__((ext_vector_type(2))) __bf16 bf2;
  typedef __attribute__((ext_vector_type(2))) float f2;
  f2 v = {a, b};
  bf2 r = __builtin_convertvector(v, bf2);
  return __builtin_bit_cast(unsigned, r);
}
DI float bfr(float a) { return bflo(pack2(a, 0.f)); }
DI bf16x8 asbf(u32x4 v) { return __builtin_bit_cast(bf16x8, v); }

DI void glds16(const void* g, void* l) {
  __builtin_amdgcn_global_load_lds((const unsigned*)g, (unsigned*)l, 16, 0, 0);
}
DI s16x4 tr_read(const char* p) {
  return __builtin_amdgcn_ds_read_tr16_b64_v4i16((__attribute__((address_space(3))) s16x4*)(p));
}
#define STAMP(k, i) do {} while (0)
DI bf16x8 cat(s16x4 lo, s16x4 hi) { return __builtin_shufflevector(lo, hi, 0, 1, 2, 3, 4, 5, 6, 7); }

__global__ __launch_bounds__(256) void k1_ln_qkv(const u16* __restrict__ x, const u16* __restrict__ wq,
                                                 const u16* __restrict__ wk, const u16* __restrict__ wv,
                                                 const u16* __restrict__ wo, u16* __restrict__ Q,
                                                 u16* __restrict__ Kf, u16* __restrict__ Vf,
                                                 u16* __restrict__ WoF) {
  __shared__ __attribute__((aligned(16))) char smem[D_ * 128];
  const int tid = threadIdx.x, l = tid & 63;
  const int w = __builtin_amdgcn_readfirstlane(tid >> 6);
  const int bx = blockIdx.x, xcd = bx & 7, ii = bx >> 3;
  const int tb = xcd + 8 * (ii >> 3), nt = ii & 7;
  const int g = l >> 4, i16 = l & 15;

  STAMP(0, 0);
  if (nt < 7) {
#pragma unroll
    for (int it = 0; it < 14; ++it) {
      const int piece = it * 4 + w;
      const int r = piece * 8 + (l >> 3);
      const int fr = ((r >> 1) & 1) | (((r >> 3) & 1) << 1);
      const int c = ((l & 7) >> 1) ^ fr;
      glds16(wq + r * D_ + nt * 64 + (2 * c + (l & 1)) * 8, smem + piece * 1024);
    }
  } else {
    const int col = (tid & 31) * 2, c16 = col & 31;
    const bool valid = c16 < DH_;
    const u16* wsrc = (col < 32 ? wk : wv) + (valid ? c16 : 0) + (tid >> 5) * DH_;
    unsigned v[56];
#pragma unroll
    for (int it = 0; it < 56; ++it) v[it] = *(const unsigned*)(wsrc + it * 8 * DH_);
#pragma unroll
    for (int it = 0; it < 56; ++it) {
      const int r = (tid >> 5) + 8 * it;
      const int fr = ((r >> 1) & 1) | (((r >> 3) & 1) << 1);
      *(unsigned*)(smem + r * 128 + (((col >> 4) ^ fr) * 32) + (col & 15) * 2) = valid ? v[it] : 0u;
    }
  }

  if (w < 2 && bx < 196) {
    const int fi = 2 * bx + w, t = fi / 14, s = fi % 14;
    const u16* src = wo + (32 * s + 8 * g) * D_ + 16 * t + i16;
    u32x4 v;
#pragma unroll
    for (int j = 0; j < 4; ++j) v[j] = (unsigned)src[(2 * j) * D_] | ((unsigned)src[(2 * j + 1) * D_] << 16);
    *(u32x4*)(WoF + ((size_t)fi * 64 + l) * 8) = v;
  }

  const int tok = tb * 64 + w * 16 + i16;
  u32x4 xf[14];
#pragma unroll
  for (int s = 0; s < 14; ++s) xf[s] = *(const u32x4*)(x + tok * D_ + 32 * s + 8 * g);
  {
    float xv[14][8];
#pragma unroll
    for (int s = 0; s < 14; ++s)
#pragma unroll
      for (int j = 0; j < 4; ++j) { xv[s][2 * j] = bflo(xf[s][j]); xv[s][2 * j + 1] = bfhi(xf[s][j]); }
    float sa[8];
#pragma unroll
    for (int j = 0; j < 8; ++j) sa[j] = xv[0][j];
#pragma unroll
    for (int s = 1; s < 14; ++s)
#pragma unroll
      for (int j = 0; j < 8; ++j) sa[j] += xv[s][j];
    float sum = ((sa[0] + sa[1]) + (sa[2] + sa[3])) + ((sa[4] + sa[5]) + (sa[6] + sa[7]));
    sum += __shfl_xor(sum, 16);
    sum += __shfl_xor(sum, 32);
    const float mean = sum * (1.0f / D_);
#pragma unroll
    for (int j = 0; j < 8; ++j) sa[j] = 0.f;
#pragma unroll
    for (int s = 0; s < 14; ++s)
#pragma unroll
      for (int j = 0; j < 8; ++j) { xv[s][j] -= mean; sa[j] = fmaf(xv[s][j], xv[s][j], sa[j]); }
    float sq = ((sa[0] + sa[1]) + (sa[2] + sa[3])) + ((sa[4] + sa[5]) + (sa[6] + sa[7]));
    sq += __shfl_xor(sq, 16);
    sq += __shfl_xor(sq, 32);
    const float mb = bfr(mean);
    const float vb = bfr(sq * (1.0f / D_));
    const float sd = bfr(__builtin_sqrtf(bfr(vb + 1e-5f)));
    const float rs = 1.0f / sd;
    const float c2 = (mean - mb) * rs;
#pragma unroll
    for (int s = 0; s < 14; ++s)
#pragma unroll
      for (int j = 0; j < 4; ++j) xf[s][j] = pack2(fmaf(xv[s][2 * j], rs, c2), fmaf(xv[s][2 * j + 1], rs, c2));
  }

  STAMP(0, 1);
  asm volatile("s_waitcnt vmcnt(0)" ::: "memory");
  __syncthreads();
  STAMP(0, 2);

  const int q4 = i16 >> 2, p4 = l & 3;
  const int fl = ((q4 >> 1) & 1) | ((g & 1) << 1);
  const char* tb0 = smem + (8 * g + q4) * 128 + p4 * 8;
  f32x4 acc[4];
#pragma unroll
  for (int t = 0; t < 4; ++t) acc[t] = (f32x4){0.f, 0.f, 0.f, 0.f};
  if (nt < 7) {
#pragma unroll
    for (int s = 0; s < 14; ++s)
#pragma unroll
      for (int t = 0; t < 4; ++t) {
        const char* p = tb0 + s * 4096 + ((t ^ fl) * 32);
        const bf16x8 wf = cat(tr_read(p), tr_read(p + 512));
        acc[t] = MFMA16(wf, asbf(xf[s]), acc[t]);
      }
    const float qs = 0.18898223650461362f * 1.4426950408889634f;
#pragma unroll
    for (int t = 0; t < 4; ++t) {
      const int n = nt * 64 + 16 * t + 4 * g;
      const int head = n / DH_, d = n - head * DH_;
      u32x2 o;
      o[0] = pack2(acc[t][0] * qs, acc[t][1] * qs);
      o[1] = pack2(acc[t][2] * qs, acc[t][3] * qs);
      *(u32x2*)(Q + ((size_t)tok * H_ + head) * 32 + d) = o;
    }
  } else {
#pragma unroll
    for (int s = 0; s < 14; ++s)
#pragma unroll
      for (int t = 0; t < 4; ++t) {
        const char* p = tb0 + s * 4096 + ((t ^ fl) * 32);
        const bf16x8 wf = cat(tr_read(p), tr_read(p + 512));
        if (t < 2) acc[t] = MFMA16(wf, asbf(xf[s]), acc[t]);
        else       acc[t] = MFMA16(asbf(xf[s]), wf, acc[t]);
      }
#pragma unroll
    for (int t = 0; t < 2; ++t) {
      const int d = 16 * t + 4 * g;
      u32x2 o;
      o[0] = pack2(acc[t][0], acc[t][1]);
      o[1] = pack2(acc[t][2], acc[t][3]);
      if (d == 28) { o[0] = 0x3F803F80u; o[1] = 0x00003F80u; }
      const int blk = tok >> 5, hh = (d & 15) >> 3, j = d & 7;
      *(u32x2*)(Kf + ((size_t)((blk * 2 + t) * 64 + (tok & 31) + 32 * hh)) * 8 + j) = o;
    }
#pragma unroll
    for (int t = 2; t < 4; ++t) {
      const int d = 16 * (t - 2) + i16;
      u32x2 o;
      o[0] = pack2(acc[t][0], acc[t][1]);
      o[1] = pack2(acc[t][2], acc[t][3]);
      if (d == 28) { o[0] = 0x3F803F80u; o[1] = 0x3F803F80u; }
      const int blk = tb * 2 + (w >> 1), s = w & 1;
      *(u32x2*)(Vf + ((size_t)((blk * 2 + s) * 64 + d + 32 * (g & 1))) * 8 + 4 * (g >> 1)) = o;
    }
  }
  STAMP(0, 3);
}

constexpr int PART_ROW = 144;
constexpr int PART_W = 128 * PART_ROW;
constexpr int K2_OBUF = 8 * PART_W;
constexpr int K2_DUMMY = K2_OBUF + 8 * D_ * 2;
constexpr int K2_SMEM = K2_DUMMY + 256;

DI float wave_sum(float v) {
  v += __builtin_bit_cast(float, __builtin_amdgcn_update_dpp(0, __builtin_bit_cast(int, v), 0x111, 0xf, 0xf, false));
  v += __builtin_bit_cast(float, __builtin_amdgcn_update_dpp(0, __builtin_bit_cast(int, v), 0x112, 0xf, 0xf, false));
  v += __builtin_bit_cast(float, __builtin_amdgcn_update_dpp(0, __builtin_bit_cast(int, v), 0x114, 0xf, 0xf, false));
  v += __builtin_bit_cast(float, __builtin_amdgcn_update_dpp(0, __builtin_bit_cast(int, v), 0x118, 0xf, 0xf, false));
  v += __builtin_bit_cast(float, __builtin_amdgcn_update_dpp(0, __builtin_bit_cast(int, v), 0x142, 0xa, 0xf, false));
  v += __builtin_bit_cast(float, __builtin_amdgcn_update_dpp(0, __builtin_bit_cast(int, v), 0x143, 0xc, 0xf, false));
  return __builtin_bit_cast(float, __builtin_amdgcn_readlane(__builtin_bit_cast(int, v), 63));
}

__global__ __launch_bounds__(512, 2) void k2_attn(const u16* __restrict__ Q, const u16* __restrict__ Kf,
                                                  const u16* __restrict__ Vf, const u16* __restrict__ WoF,
                                                  const u16* __restrict__ x, u16* __restrict__ Hout,
                                                  u16* __restrict__ HnF, const u16* __restrict__ w1,
                                                  const u16* __restrict__ w2) {
  extern __shared__ __attribute__((aligned(16))) char smem[];
  const int tid = threadIdx.x, l = tid & 63, h = l >> 5;
  const int w = __builtin_amdgcn_readfirstlane(tid >> 6);
  const int t0 = blockIdx.x * 8;

  STAMP(1, 0);
  const u32x4* kbase = (const u32x4*)Kf + (size_t)w * 8 * 128 + l;
  const u32x4* vbase = (const u32x4*)Vf + (size_t)w * 8 * 128 + l;
  u32x4 qf[4][2];
#pragma unroll
  for (int b = 0; b < 4; ++b)
#pragma unroll
    for (int s = 0; s < 2; ++s)
      qf[b][s] = *(const u32x4*)(Q + ((size_t)(t0 * H_ + 32 * b + (l & 31))) * 32 + 16 * s + 8 * h);

  f32x16 zero16;
#pragma unroll
  for (int i = 0; i < 16; ++i) zero16[i] = 0.f;

  float mval[4];
  f32x16 o[4];
  int nmax = 1;
  for (;;) {
    u32x4 k0n = kbase[0], k1n = kbase[64], v0n = vbase[0], v1n = vbase[64];
    float mx[4];
#pragma unroll
    for (int b = 0; b < 4; ++b) {
      if (h) { qf[b][1][2] = 0u; qf[b][1][3] = 0u; }
      f32x16 s = MFMA32(asbf(k0n), asbf(qf[b][0]), zero16);
      s = MFMA32(asbf(k1n), asbf(qf[b][1]), s);
      float m = s[0];
#pragma unroll
      for (int i = 1; i < 16; ++i) m = fmaxf(m, s[i]);
      mx[b] = m;
    }
    for (int kb = 1; kb < nmax; ++kb) {
      const u32x4 k0 = kbase[kb * 128], k1 = kbase[kb * 128 + 64];
#pragma unroll
      for (int b = 0; b < 4; ++b) {
        f32x16 s = MFMA32(asbf(k0), asbf(qf[b][0]), zero16);
        s = MFMA32(asbf(k1), asbf(qf[b][1]), s);
#pragma unroll
        for (int i = 0; i < 16; ++i) mx[b] = fmaxf(mx[b], s[i]);
      }
    }
#pragma unroll
    for (int b = 0; b < 4; ++b) {
      const auto sw = __builtin_amdgcn_permlane32_swap(__float_as_uint(mx[b]), __float_as_uint(mx[b]), false, false);
      const float m = fmaxf(__uint_as_float(sw[0]), __uint_as_float(sw[1]));
      mval[b] = m;
      const float nm = -m;
      const unsigned hi = __float_as_uint(nm) & 0xffff0000u;
      const float r1 = nm - __uint_as_float(hi);
      const unsigned mid = __float_as_uint(r1) & 0xffff0000u;
      const float r2 = r1 - __uint_as_float(mid);
      const unsigned lo = __float_as_uint(r2) & 0xffff0000u;
      if (h) { qf[b][1][2] = (hi >> 16) | mid; qf[b][1][3] = lo >> 16; }
      o[b] = zero16;
    }
    STAMP(1, 1);
    {
      const int xcd = blockIdx.x & 7, i = blockIdx.x >> 3;
      const u16* pf;
      if (w < 5) {
        int col = ((xcd * 49) >> 1) / 7 * 64 + w * 64;
        col = col < F_ - 64 ? col : F_ - 64;
        pf = w1 + (size_t)(64 * i + l) * F_ + col;
      } else {
        const int idx = (w - 5) * 64 + l;
        pf = w2 + (size_t)(56 * i + (idx >> 2)) * S_ + 256 * xcd + (idx & 3) * 64;
      }
      __builtin_amdgcn_global_load_lds((const unsigned*)pf, (unsigned*)(smem + K2_DUMMY), 4, 0, 0);
      if (w == 7) {
        const int idx2 = 192 + (l & 31);
        const u16* pf2 = w2 + (size_t)(56 * i + (idx2 >> 2)) * S_ + 256 * xcd + (idx2 & 3) * 64;
        __builtin_amdgcn_global_load_lds((const unsigned*)pf2, (unsigned*)(smem + K2_DUMMY), 4, 0, 0);
      }
    }
#pragma unroll 1
    for (int kb = 0; kb < 8; ++kb) {
      const u32x4 k0 = k0n, k1 = k1n, v0 = v0n, v1 = v1n;
      const int kn = (kb + 1) & 7;
      k0n = kbase[kn * 128]; k1n = kbase[kn * 128 + 64];
      v0n = vbase[kn * 128]; v1n = vbase[kn * 128 + 64];
#pragma unroll
      for (int b = 0; b < 4; ++b) {
        f32x16 s = MFMA32(asbf(k0), asbf(qf[b][0]), zero16);
        s = MFMA32(asbf(k1), asbf(qf[b][1]), s);
        u32x4 p0, p1;
#pragma unroll
        for (int i = 0; i < 4; ++i) {
          p0[i] = pack2(__builtin_amdgcn_exp2f(s[2 * i]), __builtin_amdgcn_exp2f(s[2 * i + 1]));
          p1[i] = pack2(__builtin_amdgcn_exp2f(s[8 + 2 * i]), __builtin_amdgcn_exp2f(s[8 + 2 * i + 1]));
        }
        o[b] = MFMA32(asbf(v0), asbf(p0), o[b]);
        o[b] = MFMA32(asbf(v1), asbf(p1), o[b]);
      }
    }
    bool ok = true;
#pragma unroll
    for (int b = 0; b < 4; ++b) ok = ok && (h == 0 || o[b][12] < 1.8446744e19f);
    if (nmax == 8 || __all(ok)) break;
    nmax = 8;
  }
  STAMP(1, 2);
  {
    char* pw = smem + w * PART_W;
#pragma unroll
    for (int b = 0; b < 4; ++b) {
      char* pr = pw + (32 * b + (l & 31)) * PART_ROW + 16 * h;
#pragma unroll
      for (int gq = 0; gq < 4; ++gq)
        *(f32x4*)(pr + 32 * gq) = (f32x4){o[b][4 * gq], o[b][4 * gq + 1], o[b][4 * gq + 2], o[b][4 * gq + 3]};
      if (h == 0) *(float*)(pr + 128) = mval[b];
    }
  }
  STAMP(1, 3);
  __syncthreads();
  STAMP(1, 4);
  if (w < 4) {
#pragma unroll
    for (int rep = 0; rep < 2; ++rep) {
      const int item = rep * 256 + tid;
      const int row = item >> 2, qd = item & 3;
      float mw[8], M = -3.0e38f;
#pragma unroll
      for (int ww = 0; ww < 8; ++ww) {
        mw[ww] = *(const float*)(smem + ww * PART_W + row * PART_ROW + 128);
        M = fmaxf(M, mw[ww]);
      }
      float lsum = 0.f;
      f32x4 a0 = {0.f, 0.f, 0.f, 0.f}, a1 = {0.f, 0.f, 0.f, 0.f};
#pragma unroll
      for (int ww = 0; ww < 8; ++ww) {
        const float e = __builtin_amdgcn_exp2f(mw[ww] - M);
        const char* pr = smem + ww * PART_W + row * PART_ROW;
        lsum += e * *(const float*)(pr + 112);
        a0 += e * *(const f32x4*)(pr + 32 * qd);
        a1 += e * *(const f32x4*)(pr + 32 * qd + 16);
      }
      const float inv = 1.0f / lsum;
      char* ob = smem + K2_OBUF + (row >> 4) * (D_ * 2) + (row & 15) * (DH_ * 2) + 16 * qd;
      u32x2 o0, o1;
      o0[0] = pack2(a0[0] * inv, a0[1] * inv); o0[1] = pack2(a0[2] * inv, a0[3] * inv);
      o1[0] = pack2(a1[0] * inv, a1[1] * inv); o1[1] = pack2(a1[2] * inv, a1[3] * inv);
      *(u32x2*)ob = o0;
      if (qd < 3) *(u32x2*)(ob + 8) = o1;
    }
  }
  __builtin_amdgcn_sched_barrier(0);
  const int kh = w & 1, nq = w >> 1;
  u32x4 wof[7][7];
  u16 xr[7];
  {
    const u32x4* wbase = (const u32x4*)WoF + (size_t)((7 * nq) * 14 + 7 * kh) * 64 + l;
#pragma unroll
    for (int s = 0; s < 7; ++s)
#pragma unroll
      for (int i = 0; i < 7; ++i) wof[s][i] = wbase[(i * 14 + s) * 64];
#pragma unroll
    for (int i = 0; i < 7; ++i) xr[i] = x[(t0 + w) * D_ + 64 * i + l];
  }
  __builtin_amdgcn_sched_barrier(0);
  STAMP(1, 5);
  __syncthreads();
  STAMP(1, 6);
  {
    const int g = l >> 4;
    f32x4 acc[7];
#pragma unroll
    for (int i = 0; i < 7; ++i) acc[i] = (f32x4){0.f, 0.f, 0.f, 0.f};
#pragma unroll
    for (int s = 0; s < 7; ++s) {
      const u32x4 of = *(const u32x4*)(smem + K2_OBUF + (l & 7) * (D_ * 2) + 64 * (7 * kh + s) + 16 * g);
#pragma unroll
      for (int i = 0; i < 7; ++i) acc[i] = MFMA16(asbf(wof[s][i]), asbf(of), acc[i]);
    }
    if ((l & 15) < 8) {
#pragma unroll
      for (int i = 0; i < 7; ++i)
        *(f32x4*)(smem + ((kh * 8 + (l & 15)) * D_ + 16 * (7 * nq + i) + 4 * g) * 4) = acc[i];
    }
  }
  STAMP(1, 7);
  __syncthreads();
  STAMP(1, 8);
  {
    const float* r0 = (const float*)smem + w * D_;
    const float* r1 = (const float*)smem + (8 + w) * D_;
    const int tok = t0 + w;
    float v[7];
#pragma unroll
    for (int i = 0; i < 7; ++i) {
      const int n = 64 * i + l;
      const float a = bfr(r0[n] + r1[n]);
      v[i] = bfr(bf2f(xr[i]) + a);
      Hout[tok * D_ + n] = (u16)(__float_as_uint(v[i]) >> 16);
    }
#pragma unroll
    for (int rep = 0; rep < 2; ++rep) {
      const float c = __builtin_bit_cast(float, __builtin_amdgcn_readfirstlane(__builtin_bit_cast(int, v[0])));
      float s1 = 0.f, s2 = 0.f;
#pragma unroll
      for (int i = 0; i < 7; ++i) { const float a = v[i] - c; s1 += a; s2 = fmaf(a, a, s2); }
      s1 = wave_sum(s1) * (1.0f / D_);
      s2 = wave_sum(s2) * (1.0f / D_);
      const float mean = c + s1;
      const float var = fmaxf(s2 - s1 * s1, 0.f);
      const float mb = bfr(mean);
      const float sd = bfr(__builtin_sqrtf(bfr(bfr(var) + 1e-5f)));
      const float rs = 1.0f / sd;
#pragma unroll
      for (int i = 0; i < 7; ++i) v[i] = bfr(bfr(v[i] - mb) * rs);
    }
    u16* hb = (u16*)(smem + K2_OBUF) + w * D_;
#pragma unroll
    for (int i = 0; i < 7; ++i) hb[64 * i + l] = (u16)(__float_as_uint(v[i]) >> 16);
  }
  STAMP(1, 9);
  __syncthreads();
  STAMP(1, 10);
  if (tid < D_) {
    const u16* hb = (const u16*)(smem + K2_OBUF);
    u32x4 v;
#pragma unroll
    for (int j = 0; j < 4; ++j)
      v[j] = (unsigned)hb[(2 * j) * D_ + tid] | ((unsigned)hb[(2 * j + 1) * D_ + tid] << 16);
    const int lane = ((t0 >> 3) & 3) * 16 + (tid & 15);
    *(u32x4*)(HnF + ((size_t)(((tid >> 4) * 64 + (t0 >> 5)) * 64 + lane)) * 8) = v;
  }
  STAMP(1, 11);
}

constexpr int G_SMEM = 4 * 8 * 4096;
constexpr int RED_ROW = 272;
constexpr int GDEPTH = 7;

template <int MODE>
__global__ __launch_bounds__(256, 1) void k_gemm(const u16* __restrict__ W, const u16* __restrict__ act,
                                                 const u16* __restrict__ bias, const u16* __restrict__ Hres,
                                                 u16* __restrict__ out) {
  constexpr int LDW = MODE == 0 ? F_ : S_;
  constexpr int NKS = MODE == 0 ? S_ / 32 : F_ / 32;
  constexpr int KPW = NKS / 4;
  extern __shared__ __attribute__((aligned(16))) char smem[];
  const int tid = threadIdx.x, l = tid & 63;
  const int w = __builtin_amdgcn_readfirstlane(tid >> 6);
  int dt, nt;
  {
    const int xcd = blockIdx.x & 7, i = blockIdx.x >> 3;
    int j;
    if (MODE == 0) {
      const int start = (xcd * 49) >> 1, cnt = ((xcd + 1) * 49 >> 1) - start;
      if (i >= cnt) return;
      j = start + i;
    } else {
      j = xcd * 28 + i;
    }
    nt = j / 7; dt = j - nt * 7;
  }
  char* ring = smem + w * 32768;
  const int g = l >> 4, i16 = l & 15, q4 = i16 >> 2, p4 = l & 3;
  const u16* asrc = act + ((size_t)(4 * dt) * NKS + w * KPW) * 512 + l * 8;
  const int fsrc = (l >> 4) & 1;
  const u16* wsrc = W + (size_t)(w * KPW * 32 + (l >> 3)) * LDW + 64 * nt + (l & 1) * 8;
  const int c0 = (l & 7) >> 1;

  u32x4 ep[2];
  unsigned epb[2];
#pragma unroll
  for (int rep = 0; rep < 2; ++rep) {
    const int idx = rep * 256 + tid;
    const int col = idx >> 3, r8 = (idx & 7) * 8;
    if (MODE == 0) {
      asm volatile("global_load_dwordx4 %0, %1, off" : "=v"(ep[rep]) : "v"(bias + 64 * nt + r8) : "memory");
    } else {
      asm volatile("global_load_dwordx4 %0, %1, off" : "=v"(ep[rep]) : "v"(Hres + (size_t)(64 * nt + col) * D_ + 64 * dt + r8) : "memory");
      asm volatile("global_load_ushort %0, %1, off" : "=v"(epb[rep]) : "v"(bias + 64 * nt + col) : "memory");
    }
  }

  u32x4 ar[8][4];
  auto issue = [&](int i) {
    char* slot = ring + (i & 7) * 4096;
#pragma unroll
    for (int j = 0; j < 4; ++j)
      asm volatile("global_load_dwordx4 %0, %1, off" : "=v"(ar[i & 7][j]) : "v"(asrc + ((size_t)j * NKS + i) * 512) : "memory");
#pragma unroll
    for (int j = 0; j < 4; ++j) {
      const int c = c0 ^ (fsrc | ((j & 1) << 1));
      glds16(wsrc + (size_t)(i * 32 + 8 * j) * LDW + c * 16, slot + j * 1024);
    }
  };

  f32x4 acc[4][4];
#pragma unroll
  for (int a = 0; a < 4; ++a)
#pragma unroll
    for (int b = 0; b < 4; ++b) acc[a][b] = (f32x4){0.f, 0.f, 0.f, 0.f};

  const int fl = ((q4 >> 1) & 1) | ((g & 1) << 1);
  const unsigned ring_a = (unsigned)(size_t)ring;
  unsigned trb[4];
#pragma unroll
  for (int t = 0; t < 4; ++t) trb[t] = ring_a + (8 * g + q4) * 128 + p4 * 8 + ((t ^ fl) * 32);

  STAMP(2 + MODE, 0);
#pragma unroll
  for (int i = 0; i < GDEPTH; ++i) issue(i);
  STAMP(2 + MODE, 1);

#pragma unroll
  for (int i = 0; i < KPW; ++i) {
    constexpr int dummy = 0; (void)dummy;
    const int ahead = (KPW - 1 - i) < (GDEPTH - 1) ? (KPW - 1 - i) : (GDEPTH - 1);
    const int so = (i & 7) * 4096;
    bf16x8 wf[4];
    {
      u32x2 w0l, w0h, w1l, w1h, w2l, w2h, w3l, w3h;
      asm volatile(
          "s_waitcnt vmcnt(%c18)\n\t"
          "ds_read_b64_tr_b16 %0, %12 offset:%c16\n\t"
          "ds_read_b64_tr_b16 %1, %12 offset:%c17\n\t"
          "ds_read_b64_tr_b16 %2, %13 offset:%c16\n\t"
          "ds_read_b64_tr_b16 %3, %13 offset:%c17\n\t"
          "ds_read_b64_tr_b16 %4, %14 offset:%c16\n\t"
          "ds_read_b64_tr_b16 %5, %14 offset:%c17\n\t"
          "ds_read_b64_tr_b16 %6, %15 offset:%c16\n\t"
          "ds_read_b64_tr_b16 %7, %15 offset:%c17\n\t"
          "s_waitcnt lgkmcnt(0)"
          : "=&v"(w0l), "=&v"(w0h), "=&v"(w1l), "=&v"(w1h), "=&v"(w2l), "=&v"(w2h), "=&v"(w3l), "=&v"(w3h),
            "+v"(ar[i & 7][0]), "+v"(ar[i & 7][1]), "+v"(ar[i & 7][2]), "+v"(ar[i & 7][3])
          : "v"(trb[0]), "v"(trb[1]), "v"(trb[2]), "v"(trb[3]), "i"(so), "i"(so + 512), "i"(8 * ahead)
          : "memory");
      wf[0] = asbf((u32x4){w0l[0], w0l[1], w0h[0], w0h[1]});
      wf[1] = asbf((u32x4){w1l[0], w1l[1], w1h[0], w1h[1]});
      wf[2] = asbf((u32x4){w2l[0], w2l[1], w2h[0], w2h[1]});
      wf[3] = asbf((u32x4){w3l[0], w3l[1], w3h[0], w3h[1]});
    }
    if (i + GDEPTH < KPW) issue(i + GDEPTH);
#pragma unroll
    for (int a = 0; a < 4; ++a)
#pragma unroll
      for (int b = 0; b < 4; ++b) {
        if (MODE == 0) acc[a][b] = MFMA16(wf[a], asbf(ar[i & 7][b]), acc[a][b]);
        else           acc[a][b] = MFMA16(asbf(ar[i & 7][a]), wf[b], acc[a][b]);
      }
  }
  STAMP(2 + MODE, 2);
  __syncthreads();
  STAMP(2 + MODE, 3);
  {
    char* rw = smem + w * (64 * RED_ROW);
#pragma unroll
    for (int a = 0; a < 4; ++a)
#pragma unroll
      for (int b = 0; b < 4; ++b)
        *(f32x4*)(rw + (16 * b + i16) * RED_ROW + (16 * a + 4 * g) * 4) = acc[a][b];
  }
  asm volatile("" : "+v"(ep[0]), "+v"(ep[1]));
  if (MODE == 1) asm volatile("" : "+v"(epb[0]), "+v"(epb[1]));
  __syncthreads();
#pragma unroll
  for (int rep = 0; rep < 2; ++rep) {
    const int idx = rep * 256 + tid;
    const int col = idx >> 3, r8 = (idx & 7) * 8;
    f32x4 s0 = {0.f, 0.f, 0.f, 0.f}, s1 = {0.f, 0.f, 0.f, 0.f};
#pragma unroll
    for (int ww = 0; ww < 4; ++ww) {
      const char* p = smem + ww * (64 * RED_ROW) + col * RED_ROW + r8 * 4;
      s0 += *(const f32x4*)p;
      s1 += *(const f32x4*)(p + 16);
    }
    float v[8] = {s0[0], s0[1], s0[2], s0[3], s1[0], s1[1], s1[2], s1[3]};
    u32x4 ov;
    if (MODE == 0) {
      const int d = 64 * dt + col, f = 64 * nt + r8;
      const u32x4 bv = ep[rep];
#pragma unroll
      for (int j = 0; j < 8; ++j) {
        const float bb = (j & 1) ? bfhi(bv[j >> 1]) : bflo(bv[j >> 1]);
        const float t = bfr(bfr(v[j]) + bb);
        const float y = 0.7978845608028654f * (t + 0.044715f * t * t * t);
        v[j] = t / (1.0f + __expf(-2.0f * y));
      }
#pragma unroll
      for (int j = 0; j < 4; ++j) ov[j] = pack2(v[2 * j], v[2 * j + 1]);
      const int lane = ((f & 31) >> 3) * 16 + (d & 15);
      *(u32x4*)(out + ((size_t)((d >> 4) * (F_ / 32) + (f >> 5)) * 64 + lane) * 8) = ov;
    } else {
      const int s = 64 * nt + col, d = 64 * dt + r8;
      const float bb = bf2f(epb[rep]);
      const u32x4 hv = ep[rep];
#pragma unroll
      for (int j = 0; j < 8; ++j) {
        const float hh = (j & 1) ? bfhi(hv[j >> 1]) : bflo(hv[j >> 1]);
        v[j] = hh + bfr(bfr(v[j]) + bb);
      }
#pragma unroll
      for (int j = 0; j < 4; ++j) ov[j] = pack2(v[2 * j], v[2 * j + 1]);
      *(u32x4*)(out + (size_t)s * D_ + d) = ov;
    }
  }
  STAMP(2 + MODE, 4);
}

extern "C" void kernel_launch(void* const* d_in, const int* in_sizes, int n_in, void* d_out, int out_size,
                              void* d_ws, size_t ws_size, hipStream_t stream) {
  const u16* x  = (const u16*)d_in[0];
  const u16* wq = (const u16*)d_in[1];
  const u16* wk = (const u16*)d_in[2];
  const u16* wv = (const u16*)d_in[3];
  const u16* wo = (const u16*)d_in[4];
  const u16* w1 = (const u16*)d_in[6];
  const u16* b1 = (const u16*)d_in[7];
  const u16* w2 = (const u16*)d_in[8];
  const u16* b2 = (const u16*)d_in[9];
  char* ws = (char*)d_ws;
  u16* Q   = (u16*)(ws + OFF_Q);
  u16* Kf  = (u16*)(ws + OFF_KF);
  u16* Vf  = (u16*)(ws + OFF_VF);
  u16* WoF = (u16*)(ws + OFF_WOF);
  u16* Hb  = (u16*)(ws + OFF_H);
  u16* HnF = (u16*)(ws + OFF_HNF);
  u16* UF  = (u16*)(ws + OFF_UF);

  hipFuncSetAttribute((const void*)k2_attn, hipFuncAttributeMaxDynamicSharedMemorySize, K2_SMEM);
  hipFuncSetAttribute((const void*)k_gemm<0>, hipFuncAttributeMaxDynamicSharedMemorySize, G_SMEM);
  hipFuncSetAttribute((const void*)k_gemm<1>, hipFuncAttributeMaxDynamicSharedMemorySize, G_SMEM);

  k1_ln_qkv<<<256, 256, 0, stream>>>(x, wq, wk, wv, wo, Q, Kf, Vf, WoF);
  k2_attn<<<256, 512, K2_SMEM, stream>>>(Q, Kf, Vf, WoF, x, Hb, HnF, w1, w2);
  k_gemm<0><<<8 * 25, 256, G_SMEM, stream>>>(w1, HnF, b1, nullptr, UF);
  k_gemm<1><<<8 * 28, 256, G_SMEM, stream>>>(w2, UF, b2, Hb, (u16*)d_out);
}
```

```cpp
#include <hip/hip_runtime.h>
#include <hip/hip_bf16.h>
#include <stdint.h>

typedef unsigned short u16;
typedef __attribute__((ext_vector_type(8))) short bf16x8;
typedef __attribute__((ext_vector_type(4))) short s16x4;
typedef __attribute__((ext_vector_type(4))) float f32x4;
typedef __attribute__((ext_vector_type(16))) float f32x16;
typedef __attribute__((ext_vector_type(4))) unsigned u32x4;
typedef __attribute__((ext_vector_type(2))) unsigned u32x2;

#define DI __device__ __forceinline__
#define MFMA32(a, b, c) __builtin_amdgcn_mfma_f32_32x32x16_bf16((a), (b), (c), 0, 0, 0)
#define MFMA16(a, b, c) __builtin_amdgcn_mfma_f32_16x16x32_bf16((a), (b), (c), 0, 0, 0)

constexpr int S_ = 2048, D_ = 448, H_ = 16, DH_ = 28, F_ = 1792;

constexpr size_t OFF_Q   = 0;
constexpr size_t OFF_KF  = OFF_Q + (size_t)S_ * H_ * 32 * 2;
constexpr size_t OFF_VF  = OFF_KF + (size_t)S_ * 32 * 2;
constexpr size_t OFF_WOF = OFF_VF + (size_t)S_ * 32 * 2;
constexpr size_t OFF_H   = OFF_WOF + (size_t)D_ * D_ * 2;
constexpr size_t OFF_HNF = OFF_H + (size_t)S_ * D_ * 2;
constexpr size_t OFF_UF  = OFF_HNF + (size_t)S_ * D_ * 2;

DI float bf2f(unsigned v) { return __uint_as_float(v << 16); }
DI float bflo(unsigned v) { return __uint_as_float(v << 16); }
DI float bfhi(unsigned v) { return __uint_as_float(v & 0xffff0000u); }
DI unsigned pack2(float a, float b) {
  typedef __attribute__((ext_vector_type(2))) __bf16 bf2;
  typedef __attribute__((ext_vector_type(2))) float f2;
  f2 v = {a, b};
  bf2 r = __builtin_convertvector(v, bf2);
  return __builtin_bit_cast(unsigned, r);
}
DI float bfr(float a) { return bflo(pack2(a, 0.f)); }
DI bf16x8 asbf(u32x4 v) { return __builtin_bit_cast(bf16x8, v); }

DI void glds16(const void* g, void* l) {
  __builtin_amdgcn_global_load_lds((const unsigned*)g, (unsigned*)l, 16, 0, 0);
}
DI s16x4 tr_read(const char* p) {
  return __builtin_amdgcn_ds_read_tr16_b64_v4i16((__attribute__((address_space(3))) s16x4*)(p));
}
#define STAMP(k, i) do {} while (0)
DI bf16x8 cat(s16x4 lo, s16x4 hi) { return __builtin_shufflevector(lo, hi, 0, 1, 2, 3, 4, 5, 6, 7); }

__global__ __launch_bounds__(256) void k1_ln_qkv(const u16* __restrict__ x, const u16* __restrict__ wq,
                                                 const u16* __restrict__ wk, const u16* __restrict__ wv,
                                                 const u16* __restrict__ wo, u16* __restrict__ Q,
                                                 u16* __restrict__ Kf, u16* __restrict__ Vf,
                                                 u16* __restrict__ WoF) {
  __shared__ __attribute__((aligned(16))) char smem[D_ * 128];
  const int tid = threadIdx.x, l = tid & 63;
  const int w = __builtin_amdgcn_readfirstlane(tid >> 6);
  const int bx = blockIdx.x, xcd = bx & 7, ii = bx >> 3;
  const int tb = xcd + 8 * (ii >> 3), nt = ii & 7;
  const int g = l >> 4, i16 = l & 15;

  if (bx >= 256) {
#pragma unroll
    for (int r = 0; r < 2; ++r) {
      const int fi = (bx - 256) * 4 + w + 256 * r;
      if (fi < 392) {
        const int t = fi / 14, s = fi % 14;
        const u16* src = wo + (32 * s + 8 * g) * D_ + 16 * t + i16;
        u32x4 v;
#pragma unroll
        for (int j = 0; j < 4; ++j) v[j] = (unsigned)src[(2 * j) * D_] | ((unsigned)src[(2 * j + 1) * D_] << 16);
        *(u32x4*)(WoF + ((size_t)fi * 64 + l) * 8) = v;
      }
    }
    return;
  }

  STAMP(0, 0);
  if (nt < 7) {
#pragma unroll
    for (int it = 0; it < 14; ++it) {
      const int piece = it * 4 + w;
      const int r = piece * 8 + (l >> 3);
      const int fr = ((r >> 1) & 1) | (((r >> 3) & 1) << 1);
      const int c = ((l & 7) >> 1) ^ fr;
      glds16(wq + r * D_ + nt * 64 + (2 * c + (l & 1)) * 8, smem + piece * 1024);
    }
  } else {
    const int col = (tid & 31) * 2, c16 = col & 31;
    const bool valid = c16 < DH_;
    const u16* wsrc = (col < 32 ? wk : wv) + (valid ? c16 : 0) + (tid >> 5) * DH_;
    unsigned v[56];
#pragma unroll
    for (int it = 0; it < 56; ++it) v[it] = *(const unsigned*)(wsrc + it * 8 * DH_);
#pragma unroll
    for (int it = 0; it < 56; ++it) {
      const int r = (tid >> 5) + 8 * it;
      const int fr = ((r >> 1) & 1) | (((r >> 3) & 1) << 1);
      *(unsigned*)(smem + r * 128 + (((col >> 4) ^ fr) * 32) + (col & 15) * 2) = valid ? v[it] : 0u;
    }
  }

  const int tok = tb * 64 + w * 16 + i16;
  u32x4 xf[14];
#pragma unroll
  for (int s = 0; s < 14; ++s) xf[s] = *(const u32x4*)(x + tok * D_ + 32 * s + 8 * g);
  const u32x4 ones = {0x3F803F80u, 0x3F803F80u, 0x3F803F80u, 0x3F803F80u};
  f32x4 accS = {0.f, 0.f, 0.f, 0.f}, accG = {0.f, 0.f, 0.f, 0.f};
#pragma unroll
  for (int s = 0; s < 14; ++s) {
    accS = MFMA16(asbf(ones), asbf(xf[s]), accS);
    accG = MFMA16(asbf(xf[s]), asbf(xf[s]), accG);
  }

  STAMP(0, 1);
  asm volatile("s_waitcnt vmcnt(0)" ::: "memory");
  __syncthreads();
  STAMP(0, 2);

  const int q4 = i16 >> 2, p4 = l & 3;
  const int fl = ((q4 >> 1) & 1) | ((g & 1) << 1);
  unsigned trb[4];
#pragma unroll
  for (int t = 0; t < 4; ++t) trb[t] = (unsigned)(size_t)smem + (8 * g + q4) * 128 + p4 * 8 + ((t ^ fl) * 32);
  f32x4 acc[4], accC = {0.f, 0.f, 0.f, 0.f};
#pragma unroll
  for (int t = 0; t < 4; ++t) acc[t] = (f32x4){0.f, 0.f, 0.f, 0.f};
  u32x2 wr[2][8];
#define K1_RD(S) asm volatile( \
          "ds_read_b64_tr_b16 %0, %8 offset:%c12\n\t" \
          "ds_read_b64_tr_b16 %1, %8 offset:%c13\n\t" \
          "ds_read_b64_tr_b16 %2, %9 offset:%c12\n\t" \
          "ds_read_b64_tr_b16 %3, %9 offset:%c13\n\t" \
          "ds_read_b64_tr_b16 %4, %10 offset:%c12\n\t" \
          "ds_read_b64_tr_b16 %5, %10 offset:%c13\n\t" \
          "ds_read_b64_tr_b16 %6, %11 offset:%c12\n\t" \
          "ds_read_b64_tr_b16 %7, %11 offset:%c13" \
          : "=&v"(wr[(S) & 1][0]), "=&v"(wr[(S) & 1][1]), "=&v"(wr[(S) & 1][2]), "=&v"(wr[(S) & 1][3]), \
            "=&v"(wr[(S) & 1][4]), "=&v"(wr[(S) & 1][5]), "=&v"(wr[(S) & 1][6]), "=&v"(wr[(S) & 1][7]) \
          : "v"(trb[0]), "v"(trb[1]), "v"(trb[2]), "v"(trb[3]), "i"((S) * 4096), "i"((S) * 4096 + 512) : "memory")
#define K1_WAIT(S, N) asm volatile("s_waitcnt lgkmcnt(%c8)" \
          : "+v"(wr[(S) & 1][0]), "+v"(wr[(S) & 1][1]), "+v"(wr[(S) & 1][2]), "+v"(wr[(S) & 1][3]), \
            "+v"(wr[(S) & 1][4]), "+v"(wr[(S) & 1][5]), "+v"(wr[(S) & 1][6]), "+v"(wr[(S) & 1][7]) : "i"(N) : "memory")
#define K1_WF(S, t) asbf((u32x4){wr[(S) & 1][2 * (t)][0], wr[(S) & 1][2 * (t)][1], wr[(S) & 1][2 * (t) + 1][0], wr[(S) & 1][2 * (t) + 1][1]})
  K1_RD(0);
  const bool kv = nt == 7;
#pragma unroll
  for (int s = 0; s < 14; ++s) {
    if (s < 13) { K1_RD(s + 1); K1_WAIT(s, 8); } else { K1_WAIT(s, 0); }
    acc[0] = MFMA16(K1_WF(s, 0), asbf(xf[s]), acc[0]);
    acc[1] = MFMA16(K1_WF(s, 1), asbf(xf[s]), acc[1]);
    if (!kv) {
      acc[2] = MFMA16(K1_WF(s, 2), asbf(xf[s]), acc[2]);
      acc[3] = MFMA16(K1_WF(s, 3), asbf(xf[s]), acc[3]);
    } else {
      acc[2] = MFMA16(asbf(xf[s]), K1_WF(s, 2), acc[2]);
      acc[3] = MFMA16(asbf(xf[s]), K1_WF(s, 3), acc[3]);
    }
    const bf16x8 wsel = w == 0 ? K1_WF(s, 0) : w == 1 ? K1_WF(s, 1) : w == 2 ? K1_WF(s, 2) : K1_WF(s, 3);
    accC = MFMA16(asbf(ones), wsel, accC);
  }
  __syncthreads();
  float* csb = (float*)smem;
  if (g == 0) csb[16 * w + i16] = accC[0];
  float mb, rs;
  {
    const int r3 = l & 3;
    const float cand = r3 == 0 ? accG[0] : r3 == 1 ? accG[1] : r3 == 2 ? accG[2] : accG[3];
    const float g2 = __shfl(cand, i16 + 16 * (i16 >> 2));
    const float mean = accS[0] * (1.0f / D_);
    const float var = fmaxf(g2 * (1.0f / D_) - mean * mean, 0.f);
    mb = bfr(mean);
    rs = __builtin_amdgcn_rcpf(bfr(__builtin_amdgcn_sqrtf(bfr(bfr(var) + 1e-5f))));
  }
  __syncthreads();
  if (!kv) {
    const float qs = rs * (0.18898223650461362f * 1.4426950408889634f);
#pragma unroll
    for (int t = 0; t < 4; ++t) {
      const int n = nt * 64 + 16 * t + 4 * g;
      const int head = n / DH_, d = n - head * DH_;
      const f32x4 cs = *(const f32x4*)(csb + 16 * t + 4 * g);
      u32x2 o;
      o[0] = pack2((acc[t][0] - mb * cs[0]) * qs, (acc[t][1] - mb * cs[1]) * qs);
      o[1] = pack2((acc[t][2] - mb * cs[2]) * qs, (acc[t][3] - mb * cs[3]) * qs);
      *(u32x2*)(Q + ((size_t)tok * H_ + head) * 32 + d) = o;
    }
  } else {
#pragma unroll
    for (int t = 0; t < 2; ++t) {
      const int d = 16 * t + 4 * g;
      const f32x4 cs = *(const f32x4*)(csb + d);
      u32x2 o;
      o[0] = pack2((acc[t][0] - mb * cs[0]) * rs, (acc[t][1] - mb * cs[1]) * rs);
      o[1] = pack2((acc[t][2] - mb * cs[2]) * rs, (acc[t][3] - mb * cs[3]) * rs);
      if (d == 28) { o[0] = 0x3F803F80u; o[1] = 0x00003F80u; }
      const int blk = tok >> 5, hh = (d & 15) >> 3, j = d & 7;
      *(u32x2*)(Kf + ((size_t)((blk * 2 + t) * 64 + (tok & 31) + 32 * hh)) * 8 + j) = o;
    }
    float mbk[4], rsk[4];
#pragma unroll
    for (int r = 0; r < 4; ++r) { mbk[r] = __shfl(mb, 4 * g + r); rsk[r] = __shfl(rs, 4 * g + r); }
#pragma unroll
    for (int t = 2; t < 4; ++t) {
      const int d = 16 * (t - 2) + i16;
      const float cs = csb[16 * t + i16];
      u32x2 o;
      o[0] = pack2((acc[t][0] - mbk[0] * cs) * rsk[0], (acc[t][1] - mbk[1] * cs) * rsk[1]);
      o[1] = pack2((acc[t][2] - mbk[2] * cs) * rsk[2], (acc[t][3] - mbk[3] * cs) * rsk[3]);
      if (d == 28) { o[0] = 0x3F803F80u; o[1] = 0x3F803F80u; }
      const int blk = tb * 2 + (w >> 1), s = w & 1;
      *(u32x2*)(Vf + ((size_t)((blk * 2 + s) * 64 + d + 32 * (g & 1))) * 8 + 4 * (g >> 1)) = o;
    }
  }
  STAMP(0, 3);
}

constexpr int PART_ROW = 144;
constexpr int PART_W = 128 * PART_ROW;
constexpr int K2_OBUF = 8 * PART_W;
constexpr int K2_DUMMY = K2_OBUF + 8 * D_ * 2;
constexpr int K2_SMEM = K2_DUMMY + 256;

DI float wave_sum(float v) {
  v += __builtin_bit_cast(float, __builtin_amdgcn_update_dpp(0, __builtin_bit_cast(int, v), 0x111, 0xf, 0xf, false));
  v += __builtin_bit_cast(float, __builtin_amdgcn_update_dpp(0, __builtin_bit_cast(int, v), 0x112, 0xf, 0xf, false));
  v += __builtin_bit_cast(float, __builtin_amdgcn_update_dpp(0, __builtin_bit_cast(int, v), 0x114, 0xf, 0xf, false));
  v += __builtin_bit_cast(float, __builtin_amdgcn_update_dpp(0, __builtin_bit_cast(int, v), 0x118, 0xf, 0xf, false));
  v += __builtin_bit_cast(float, __builtin_amdgcn_update_dpp(0, __builtin_bit_cast(int, v), 0x142, 0xa, 0xf, false));
  v += __builtin_bit_cast(float, __builtin_amdgcn_update_dpp(0, __builtin_bit_cast(int, v), 0x143, 0xc, 0xf, false));
  return __builtin_bit_cast(float, __builtin_amdgcn_readlane(__builtin_bit_cast(int, v), 63));
}

__global__ __launch_bounds__(512, 2) void k2_attn(const u16* __restrict__ Q, const u16* __restrict__ Kf,
                                                  const u16* __restrict__ Vf, const u16* __restrict__ WoF,
                                                  const u16* __restrict__ x, u16* __restrict__ Hout,
                                                  u16* __restrict__ HnF, const u16* __restrict__ w1,
                                                  const u16* __restrict__ w2) {
  extern __shared__ __attribute__((aligned(16))) char smem[];
  const int tid = threadIdx.x, l = tid & 63, h = l >> 5;
  const int w = __builtin_amdgcn_readfirstlane(tid >> 6);
  const int t0 = blockIdx.x * 8;

  STAMP(1, 0);
  if (w < 4) __builtin_amdgcn_s_setprio(2);
  const u32x4* kbase = (const u32x4*)Kf + (size_t)w * 8 * 128 + l;
  const u32x4* vbase = (const u32x4*)Vf + (size_t)w * 8 * 128 + l;
  u32x4 qf[4][2];
#pragma unroll
  for (int b = 0; b < 4; ++b)
#pragma unroll
    for (int s = 0; s < 2; ++s)
      qf[b][s] = *(const u32x4*)(Q + ((size_t)(t0 * H_ + 32 * b + (l & 31))) * 32 + 16 * s + 8 * h);

  f32x16 zero16;
#pragma unroll
  for (int i = 0; i < 16; ++i) zero16[i] = 0.f;

  float mval[4];
  f32x16 o[4];
  int nmax = 1;
  for (;;) {
    u32x4 k0n = kbase[0], k1n = kbase[64], v0n = vbase[0], v1n = vbase[64];
    u32x4 k0m = kbase[128], k1m = kbase[192], v0m = vbase[128], v1m = vbase[192];
    float mx[4];
#pragma unroll
    for (int b = 0; b < 4; ++b) {
      if (h) { qf[b][1][2] = 0u; qf[b][1][3] = 0u; }
      f32x16 s = MFMA32(asbf(k0n), asbf(qf[b][0]), zero16);
      s = MFMA32(asbf(k1n), asbf(qf[b][1]), s);
      float m = s[0];
#pragma unroll
      for (int i = 1; i < 16; ++i) m = fmaxf(m, s[i]);
      mx[b] = m;
    }
    for (int kb = 1; kb < nmax; ++kb) {
      const u32x4 k0 = kbase[kb * 128], k1 = kbase[kb * 128 + 64];
#pragma unroll
      for (int b = 0; b < 4; ++b) {
        f32x16 s = MFMA32(asbf(k0), asbf(qf[b][0]), zero16);
        s = MFMA32(asbf(k1), asbf(qf[b][1]), s);
#pragma unroll
        for (int i = 0; i < 16; ++i) mx[b] = fmaxf(mx[b], s[i]);
      }
    }
#pragma unroll
    for (int b = 0; b < 4; ++b) {
      const auto sw = __builtin_amdgcn_permlane32_swap(__float_as_uint(mx[b]), __float_as_uint(mx[b]), false, false);
      const float m = fmaxf(__uint_as_float(sw[0]), __uint_as_float(sw[1]));
      mval[b] = m;
      const float nm = -m;
      const unsigned hi = __float_as_uint(nm) & 0xffff0000u;
      const float r1 = nm - __uint_as_float(hi);
      const unsigned mid = __float_as_uint(r1) & 0xffff0000u;
      const float r2 = r1 - __uint_as_float(mid);
      const unsigned lo = __float_as_uint(r2) & 0xffff0000u;
      if (h) { qf[b][1][2] = (hi >> 16) | mid; qf[b][1][3] = lo >> 16; }
      o[b] = zero16;
    }
    STAMP(1, 1);
#define SB() __builtin_amdgcn_sched_barrier(0)
#define EXP4(S, i) do { S[i] = __builtin_amdgcn_exp2f(S[i]); S[i + 1] = __builtin_amdgcn_exp2f(S[i + 1]); \
                        S[i + 2] = __builtin_amdgcn_exp2f(S[i + 2]); S[i + 3] = __builtin_amdgcn_exp2f(S[i + 3]); } while (0)
#define STEP(SC, SN, B, BN, BP, KN0, KN1, V0, VP1) do { \
      u32x4 p0; \
      o[BP] = MFMA32(asbf(VP1), asbf(pp), o[BP]); SB(); \
      EXP4(SC, 0); SB(); \
      SN = MFMA32(asbf(KN0), asbf(qf[BN][0]), zero16); SB(); \
      EXP4(SC, 4); \
      p0[0] = pack2(SC[0], SC[1]); p0[1] = pack2(SC[2], SC[3]); p0[2] = pack2(SC[4], SC[5]); p0[3] = pack2(SC[6], SC[7]); SB(); \
      asm volatile("" :: "v"(pp)); \
      SN = MFMA32(asbf(KN1), asbf(qf[BN][1]), SN); SB(); \
      EXP4(SC, 8); SB(); \
      o[B] = MFMA32(asbf(V0), asbf(p0), o[B]); SB(); \
      EXP4(SC, 12); \
      pp[0] = pack2(SC[8], SC[9]); pp[1] = pack2(SC[10], SC[11]); pp[2] = pack2(SC[12], SC[13]); pp[3] = pack2(SC[14], SC[15]); SB(); \
      asm volatile("" :: "v"(p0)); \
    } while (0)
    {
      u32x4 kA0 = k0n, kA1 = k1n, vA0 = v0n, vA1 = v1n;
      u32x4 kB0 = k0m, kB1 = k1m, vB0 = v0m, vB1 = v1m;
      u32x4 kC0, kC1, vC0, vC1 = v1n;
      u32x4 pp = {0u, 0u, 0u, 0u};
      f32x16 sa, sb;
      sa = MFMA32(asbf(kA0), asbf(qf[0][0]), zero16);
      sa = MFMA32(asbf(kA1), asbf(qf[0][1]), sa);
#define BLOCK(KB, X, Y, Z, SA, SB_) do { \
        const int kn_ = ((KB) + 2) & 7; \
        k##Z##0 = kbase[kn_ * 128]; k##Z##1 = kbase[kn_ * 128 + 64]; v##Z##0 = vbase[kn_ * 128]; \
        SB(); \
        STEP(SA, SB_, 0, 1, 3, k##X##0, k##X##1, v##X##0, v##Z##1); \
        v##Z##1 = vbase[kn_ * 128 + 64]; \
        SB(); \
        STEP(SB_, SA, 1, 2, 0, k##X##0, k##X##1, v##X##0, v##X##1); \
        STEP(SA, SB_, 2, 3, 1, k##X##0, k##X##1, v##X##0, v##X##1); \
        STEP(SB_, SA, 3, 0, 2, k##Y##0, k##Y##1, v##X##0, v##X##1); \
      } while (0)
#pragma unroll 1
      for (int kb = 0; kb < 6; kb += 3) {
        BLOCK(kb, A, B, C, sa, sb);
        if (kb == 0 || w == 0) {
          const int job = kb == 0 ? w : 8;
          const int xcd = blockIdx.x & 7, bi = blockIdx.x >> 3;
          const u16* pf;
          if (job < 5) {
            const int idx = job * 64 + l, row = idx / 5, ln = idx - row * 5;
            int col = ((xcd * 49) >> 1) / 7 * 64 + ln * 64;
            col = col < F_ - 64 ? col : F_ - 64;
            pf = w1 + (size_t)(64 * bi + row) * F_ + col;
          } else {
            int idx = (job - 5) * 64 + l;
            idx = idx < 224 ? idx : 223;
            pf = w2 + (size_t)(56 * bi + (idx >> 2)) * S_ + 256 * xcd + (idx & 3) * 64;
          }
          const unsigned dm = (unsigned)(size_t)(smem + K2_DUMMY);
          unsigned keep;
          asm volatile("s_mov_b32 %0, m0\n\ts_mov_b32 m0, %2\n\ts_nop 0\n\tglobal_load_lds_dword %1, off\n\ts_mov_b32 m0, %0"
                       : "=&s"(keep) : "v"(pf), "s"(dm) : "memory");
        }
        BLOCK(kb + 1, B, C, A, sa, sb);
        BLOCK(kb + 2, C, A, B, sa, sb);
      }
      BLOCK(6, A, B, C, sa, sb);
      BLOCK(7, B, C, A, sa, sb);
      o[3] = MFMA32(asbf(vB1), asbf(pp), o[3]);
    }
    bool ok = true;
#pragma unroll
    for (int b = 0; b < 4; ++b) ok = ok && (h == 0 || o[b][12] < 1.8446744e19f);
    if (nmax == 8 || __all(ok)) break;
    nmax = 8;
  }
  STAMP(1, 2);
  {
    char* pw = smem + w * PART_W;
#pragma unroll
    for (int b = 0; b < 4; ++b) {
      char* pr = pw + (32 * b + (l & 31)) * PART_ROW + 16 * h;
#pragma unroll
      for (int gq = 0; gq < 4; ++gq)
        *(f32x4*)(pr + 32 * gq) = (f32x4){o[b][4 * gq], o[b][4 * gq + 1], o[b][4 * gq + 2], o[b][4 * gq + 3]};
      if (h == 0) *(float*)(pr + 128) = mval[b];
    }
  }
  const int role = (w + (blockIdx.x >> 3)) & 7;
  const int kh = role & 1, nq = role >> 1;
  u16 xr[7];
  const u32x4* wbase = (const u32x4*)WoF + (size_t)((7 * nq) * 14 + 7 * kh) * 64 + l;
  __builtin_amdgcn_s_setprio(0);
#define LOAD_WOF(S0, S1) do { \
    _Pragma("unroll") for (int s = S0; s < S1; ++s) \
      _Pragma("unroll") for (int i = 0; i < 7; ++i) wof[s][i] = wbase[(i * 14 + s) * 64]; \
  } while (0)
#define LOAD_XR() do { \
    _Pragma("unroll") for (int i = 0; i < 7; ++i) xr[i] = x[(t0 + w) * D_ + 64 * i + l]; \
  } while (0)
#define WO_GEMM() do { \
    const int g = l >> 4; \
    f32x4 acc[7]; \
    _Pragma("unroll") for (int i = 0; i < 7; ++i) acc[i] = (f32x4){0.f, 0.f, 0.f, 0.f}; \
    _Pragma("unroll") for (int s = 0; s < 7; ++s) { \
      const u32x4 of = *(const u32x4*)(smem + K2_OBUF + (l & 7) * (D_ * 2) + 64 * (7 * kh + s) + 16 * g); \
      _Pragma("unroll") for (int i = 0; i < 7; ++i) acc[i] = MFMA16(asbf(wof[s][i]), asbf(of), acc[i]); \
    } \
    if ((l & 15) < 8) { \
      _Pragma("unroll") for (int i = 0; i < 7; ++i) \
        *(f32x4*)(smem + ((kh * 8 + (l & 15)) * D_ + 16 * (7 * nq + i) + 4 * g) * 4) = acc[i]; \
    } \
  } while (0)
  if (w < 4) {
    u32x4 wof[7][7];
    LOAD_WOF(0, 6);
    __builtin_amdgcn_sched_barrier(0);
    STAMP(1, 3);
    __syncthreads();
    STAMP(1, 4);
#pragma unroll 1
    for (int rep = 0; rep < 2; ++rep) {
      const int item = rep * 256 + tid;
      const int row = item >> 2, qd = item & 3;
      const char* pr0 = smem + row * PART_ROW;
      float M = -3.0e38f;
#pragma unroll
      for (int ww = 0; ww < 8; ++ww) M = fmaxf(M, *(const float*)(pr0 + ww * PART_W + 128));
      float lsum = 0.f;
      f32x4 a0 = {0.f, 0.f, 0.f, 0.f}, a1 = {0.f, 0.f, 0.f, 0.f};
#pragma unroll
      for (int ww = 0; ww < 8; ++ww) {
        const char* pr = pr0 + ww * PART_W;
        const float e = __builtin_amdgcn_exp2f(*(const float*)(pr + 128) - M);
        lsum += e * *(const float*)(pr + 112);
        a0 += e * *(const f32x4*)(pr + 32 * qd);
        a1 += e * *(const f32x4*)(pr + 32 * qd + 16);
      }
      const float inv = __builtin_amdgcn_rcpf(lsum);
      char* ob = smem + K2_OBUF + (row >> 4) * (D_ * 2) + (row & 15) * (DH_ * 2) + 16 * qd;
      u32x2 o0, o1;
      o0[0] = pack2(a0[0] * inv, a0[1] * inv); o0[1] = pack2(a0[2] * inv, a0[3] * inv);
      o1[0] = pack2(a1[0] * inv, a1[1] * inv); o1[1] = pack2(a1[2] * inv, a1[3] * inv);
      *(u32x2*)ob = o0;
      if (qd < 3) *(u32x2*)(ob + 8) = o1;
    }
    LOAD_WOF(6, 7);
    LOAD_XR();
    __builtin_amdgcn_sched_barrier(0);
    STAMP(1, 5);
    __syncthreads();
    STAMP(1, 6);
    WO_GEMM();
  } else {
    STAMP(1, 3);
    __syncthreads();
    STAMP(1, 4);
    u32x4 wof[7][7];
    LOAD_WOF(0, 7);
    LOAD_XR();
    __builtin_amdgcn_sched_barrier(0);
    STAMP(1, 5);
    __syncthreads();
    STAMP(1, 6);
    WO_GEMM();
  }
  STAMP(1, 7);
  __syncthreads();
  STAMP(1, 8);
  {
    const float* r0 = (const float*)smem + w * D_;
    const float* r1 = (const float*)smem + (8 + w) * D_;
    const int tok = t0 + w;
    float v[7];
#pragma unroll
    for (int i = 0; i < 7; ++i) {
      const int n = 64 * i + l;
      const float a = bfr(r0[n] + r1[n]);
      v[i] = bfr(bf2f(xr[i]) + a);
      Hout[tok * D_ + n] = (u16)(__float_as_uint(v[i]) >> 16);
    }
#pragma unroll
    for (int rep = 0; rep < 2; ++rep) {
      const float c = __builtin_bit_cast(float, __builtin_amdgcn_readfirstlane(__builtin_bit_cast(int, v[0])));
      float s1 = 0.f, s2 = 0.f;
#pragma unroll
      for (int i = 0; i < 7; ++i) { const float a = v[i] - c; s1 += a; s2 = fmaf(a, a, s2); }
      s1 = wave_sum(s1) * (1.0f / D_);
      s2 = wave_sum(s2) * (1.0f / D_);
      const float mean = c + s1;
      const float var = fmaxf(s2 - s1 * s1, 0.f);
      const float mb = bfr(mean);
      const float sd = bfr(__builtin_amdgcn_sqrtf(bfr(bfr(var) + 1e-5f)));
      const float rs = __builtin_amdgcn_rcpf(sd);
#pragma unroll
      for (int i = 0; i < 7; ++i) v[i] = bfr(bfr(v[i] - mb) * rs);
    }
    u16* hb = (u16*)(smem + K2_OBUF) + w * D_;
#pragma unroll
    for (int i = 0; i < 7; ++i) hb[64 * i + l] = (u16)(__float_as_uint(v[i]) >> 16);
  }
  STAMP(1, 9);
  __syncthreads();
  STAMP(1, 10);
  if (tid < D_) {
    const u16* hb = (const u16*)(smem + K2_OBUF);
    u32x4 v;
#pragma unroll
    for (int j = 0; j < 4; ++j)
      v[j] = (unsigned)hb[(2 * j) * D_ + tid] | ((unsigned)hb[(2 * j + 1) * D_ + tid] << 16);
    const int lane = ((t0 >> 3) & 3) * 16 + (tid & 15);
    *(u32x4*)(HnF + ((size_t)(((tid >> 4) * 64 + (t0 >> 5)) * 64 + lane)) * 8) = v;
  }
  STAMP(1, 11);
}

constexpr int G_SMEM = 8 * 16384;
constexpr int GDEPTH = 4;

template <int MODE>
__global__ __launch_bounds__(512, 2) void k_gemm(const u16* __restrict__ W, const u16* __restrict__ act,
                                                 const u16* __restrict__ bias, const u16* __restrict__ Hres,
                                                 u16* __restrict__ out) {
  constexpr int LDW = MODE == 0 ? F_ : S_;
  constexpr int NKS = MODE == 0 ? S_ / 32 : F_ / 32;
  constexpr int KPW = NKS / 8;
  extern __shared__ __attribute__((aligned(16))) char smem[];
  const int tid = threadIdx.x, l = tid & 63;
  const int w = __builtin_amdgcn_readfirstlane(tid >> 6);
  int dt, nt;
  {
    const int xcd = blockIdx.x & 7, i = blockIdx.x >> 3;
    int j;
    if (MODE == 0) {
      const int start = (xcd * 49) >> 1, cnt = ((xcd + 1) * 49 >> 1) - start;
      if (i >= cnt) return;
      j = start + i;
    } else {
      j = xcd * 28 + i;
    }
    nt = j / 7; dt = j - nt * 7;
  }
  char* ring = smem + w * (GDEPTH * 4096);
  const int g = l >> 4, i16 = l & 15, q4 = i16 >> 2, p4 = l & 3;
  const u16* asrc = act + ((size_t)(4 * dt) * NKS + w * KPW) * 512 + l * 8;
  const int fsrc = (l >> 4) & 1;
  const u16* wsrc = W + (size_t)(w * KPW * 32 + (l >> 3)) * LDW + 64 * nt + (l & 1) * 8;
  const int c0 = (l & 7) >> 1;
  const int ecol = tid >> 3, er8 = (tid & 7) * 8;

  u32x4 ep;
  unsigned epb = 0;
  if (MODE == 0) {
    asm volatile("global_load_dwordx4 %0, %1, off" : "=v"(ep) : "v"(bias + 64 * nt + er8) : "memory");
  } else {
    asm volatile("global_load_dwordx4 %0, %1, off" : "=v"(ep) : "v"(Hres + (size_t)(64 * nt + ecol) * D_ + 64 * dt + er8) : "memory");
    asm volatile("global_load_ushort %0, %1, off" : "=v"(epb) : "v"(bias + 64 * nt + ecol) : "memory");
  }

  u32x4 ar[GDEPTH][4];
  auto issue = [&](int i) {
    char* slot = ring + (i % GDEPTH) * 4096;
#pragma unroll
    for (int j = 0; j < 4; ++j) {
      const int c = c0 ^ (fsrc | ((j & 1) << 1));
      glds16(wsrc + (size_t)(i * 32 + 8 * j) * LDW + c * 16, slot + j * 1024);
    }
#pragma unroll
    for (int j = 0; j < 4; ++j)
      asm volatile("global_load_dwordx4 %0, %1, off" : "=v"(ar[i % GDEPTH][j]) : "v"(asrc + ((size_t)j * NKS + i) * 512) : "memory");
  };

  f32x4 acc[4][4];
#pragma unroll
  for (int a = 0; a < 4; ++a)
#pragma unroll
    for (int b = 0; b < 4; ++b) acc[a][b] = (f32x4){0.f, 0.f, 0.f, 0.f};

  const int fl = ((q4 >> 1) & 1) | ((g & 1) << 1);
  const unsigned ring_a = (unsigned)(size_t)ring;
  unsigned trb[4];
#pragma unroll
  for (int t = 0; t < 4; ++t) trb[t] = ring_a + (8 * g + q4) * 128 + p4 * 8 + ((t ^ fl) * 32);

  STAMP(2 + MODE, 0);
#pragma unroll
  for (int i = 0; i < GDEPTH; ++i) issue(i);
  STAMP(2 + MODE, 1);

#pragma unroll
  for (int i = 0; i < KPW; ++i) {
    const int ahead = (KPW - 1 - i) < (GDEPTH - 1) ? (KPW - 1 - i) : (GDEPTH - 1);
    const int so = (i % GDEPTH) * 4096;
    bf16x8 wf[4];
    u32x4 a0 = ar[i % GDEPTH][0], a1 = ar[i % GDEPTH][1], a2 = ar[i % GDEPTH][2], a3 = ar[i % GDEPTH][3];
    {
      u32x2 w0l, w0h, w1l, w1h, w2l, w2h, w3l, w3h;
      asm volatile(
          "s_waitcnt vmcnt(%c18)\n\t"
          "ds_read_b64_tr_b16 %0, %12 offset:%c16\n\t"
          "ds_read_b64_tr_b16 %1, %12 offset:%c17\n\t"
          "ds_read_b64_tr_b16 %2, %13 offset:%c16\n\t"
          "ds_read_b64_tr_b16 %3, %13 offset:%c17\n\t"
          "ds_read_b64_tr_b16 %4, %14 offset:%c16\n\t"
          "ds_read_b64_tr_b16 %5, %14 offset:%c17\n\t"
          "ds_read_b64_tr_b16 %6, %15 offset:%c16\n\t"
          "ds_read_b64_tr_b16 %7, %15 offset:%c17\n\t"
          "s_waitcnt lgkmcnt(0)"
          : "=&v"(w0l), "=&v"(w0h), "=&v"(w1l), "=&v"(w1h), "=&v"(w2l), "=&v"(w2h), "=&v"(w3l), "=&v"(w3h),
            "+v"(a0), "+v"(a1), "+v"(a2), "+v"(a3)
          : "v"(trb[0]), "v"(trb[1]), "v"(trb[2]), "v"(trb[3]), "i"(so), "i"(so + 512), "i"(8 * ahead)
          : "memory");
      wf[0] = asbf((u32x4){w0l[0], w0l[1], w0h[0], w0h[1]});
      wf[1] = asbf((u32x4){w1l[0], w1l[1], w1h[0], w1h[1]});
      wf[2] = asbf((u32x4){w2l[0], w2l[1], w2h[0], w2h[1]});
      wf[3] = asbf((u32x4){w3l[0], w3l[1], w3h[0], w3h[1]});
    }
    if (i + GDEPTH < KPW) issue(i + GDEPTH);
    const bf16x8 af[4] = {asbf(a0), asbf(a1), asbf(a2), asbf(a3)};
#pragma unroll
    for (int a = 0; a < 4; ++a)
#pragma unroll
      for (int b = 0; b < 4; ++b) {
        if (MODE == 0) acc[a][b] = MFMA16(wf[a], af[b], acc[a][b]);
        else           acc[a][b] = MFMA16(af[a], wf[b], acc[a][b]);
      }
  }
  STAMP(2 + MODE, 2);
  {
#pragma unroll
    for (int a = 0; a < 4; ++a)
#pragma unroll
      for (int b = 0; b < 4; ++b)
        *(f32x4*)(ring + (16 * b + i16) * 256 + (((4 * a + g) ^ i16) * 16)) = acc[a][b];
  }
  asm volatile("" : "+v"(ep), "+v"(epb));
  STAMP(2 + MODE, 3);
  __syncthreads();
  {
    f32x4 s0 = {0.f, 0.f, 0.f, 0.f}, s1 = {0.f, 0.f, 0.f, 0.f};
    const int ch = (tid & 7) * 2;
    const char* p0 = smem + ecol * 256 + ((ch ^ (ecol & 15)) * 16);
    const char* p1 = smem + ecol * 256 + (((ch + 1) ^ (ecol & 15)) * 16);
#pragma unroll
    for (int ww = 0; ww < 8; ++ww) {
      s0 += *(const f32x4*)(p0 + ww * 16384);
      s1 += *(const f32x4*)(p1 + ww * 16384);
    }
    float v[8] = {s0[0], s0[1], s0[2], s0[3], s1[0], s1[1], s1[2], s1[3]};
    u32x4 ov;
    if (MODE == 0) {
      const int d = 64 * dt + ecol, f = 64 * nt + er8;
#pragma unroll
      for (int j = 0; j < 8; ++j) {
        const float bb = (j & 1) ? bfhi(ep[j >> 1]) : bflo(ep[j >> 1]);
        const float t = bfr(bfr(v[j]) + bb);
        const float y = 0.7978845608028654f * (t + 0.044715f * t * t * t);
        v[j] = t * __builtin_amdgcn_rcpf(1.0f + __expf(-2.0f * y));
      }
#pragma unroll
      for (int j = 0; j < 4; ++j) ov[j] = pack2(v[2 * j], v[2 * j + 1]);
      const int lane = ((f & 31) >> 3) * 16 + (d & 15);
      *(u32x4*)(out + ((size_t)((d >> 4) * (F_ / 32) + (f >> 5)) * 64 + lane) * 8) = ov;
    } else {
      const int s = 64 * nt + ecol, d = 64 * dt + er8;
      const float bb = bf2f(epb);
#pragma unroll
      for (int j = 0; j < 8; ++j) {
        const float hh = (j & 1) ? bfhi(ep[j >> 1]) : bflo(ep[j >> 1]);
        v[j] = hh + bfr(bfr(v[j]) + bb);
      }
#pragma unroll
      for (int j = 0; j < 4; ++j) ov[j] = pack2(v[2 * j], v[2 * j + 1]);
      *(u32x4*)(out + (size_t)s * D_ + d) = ov;
    }
  }
  STAMP(2 + MODE, 4);
}

extern "C" void kernel_launch(void* const* d_in, const int* in_sizes, int n_in, void* d_out, int out_size,
                              void* d_ws, size_t ws_size, hipStream_t stream) {
  const u16* x  = (const u16*)d_in[0];
  const u16* wq = (const u16*)d_in[1];
  const u16* wk = (const u16*)d_in[2];
  const u16* wv = (const u16*)d_in[3];
  const u16* wo = (const u16*)d_in[4];
  const u16* w1 = (const u16*)d_in[6];
  const u16* b1 = (const u16*)d_in[7];
  const u16* w2 = (const u16*)d_in[8];
  const u16* b2 = (const u16*)d_in[9];
  char* ws = (char*)d_ws;
  u16* Q   = (u16*)(ws + OFF_Q);
  u16* Kf  = (u16*)(ws + OFF_KF);
  u16* Vf  = (u16*)(ws + OFF_VF);
  u16* WoF = (u16*)(ws + OFF_WOF);
  u16* Hb  = (u16*)(ws + OFF_H);
  u16* HnF = (u16*)(ws + OFF_HNF);
  u16* UF  = (u16*)(ws + OFF_UF);

  hipFuncSetAttribute((const void*)k2_attn, hipFuncAttributeMaxDynamicSharedMemorySize, K2_SMEM);
  hipFuncSetAttribute((const void*)k_gemm<0>, hipFuncAttributeMaxDynamicSharedMemorySize, G_SMEM);
  hipFuncSetAttribute((const void*)k_gemm<1>, hipFuncAttributeMaxDynamicSharedMemorySize, G_SMEM);

  k1_ln_qkv<<<320, 256, 0, stream>>>(x, wq, wk, wv, wo, Q, Kf, Vf, WoF);
  k2_attn<<<256, 512, K2_SMEM, stream>>>(Q, Kf, Vf, WoF, x, Hb, HnF, w1, w2);
  k_gemm<0><<<8 * 25, 512, G_SMEM, stream>>>(w1, HnF, b1, nullptr, UF);
  k_gemm<1><<<8 * 28, 512, G_SMEM, stream>>>(w2, UF, b2, Hb, (u16*)d_out);
}
```

```cpp
#include <hip/hip_runtime.h>
#include <hip/hip_bf16.h>
#include <stdint.h>

typedef unsigned short u16;
typedef __attribute__((ext_vector_type(8))) short bf16x8;
typedef __attribute__((ext_vector_type(4))) short s16x4;
typedef __attribute__((ext_vector_type(4))) float f32x4;
typedef __attribute__((ext_vector_type(16))) float f32x16;
typedef __attribute__((ext_vector_type(4))) unsigned u32x4;
typedef __attribute__((ext_vector_type(2))) unsigned u32x2;

#define DI __device__ __forceinline__
#define MFMA32(a, b, c) __builtin_amdgcn_mfma_f32_32x32x16_bf16((a), (b), (c), 0, 0, 0)
#define MFMA16(a, b, c) __builtin_amdgcn_mfma_f32_16x16x32_bf16((a), (b), (c), 0, 0, 0)

constexpr int S_ = 2048, D_ = 448, H_ = 16, DH_ = 28, F_ = 1792;

constexpr size_t OFF_Q   = 0;
constexpr size_t OFF_KF  = OFF_Q + (size_t)S_ * H_ * 32 * 2;
constexpr size_t OFF_VF  = OFF_KF + (size_t)S_ * 32 * 2;
constexpr size_t OFF_WOF = OFF_VF + (size_t)S_ * 32 * 2;
constexpr size_t OFF_H   = OFF_WOF + (size_t)D_ * D_ * 2;
constexpr size_t OFF_HNF = OFF_H + (size_t)S_ * D_ * 2;
constexpr size_t OFF_UF  = OFF_HNF + (size_t)S_ * D_ * 2;
constexpr size_t OFF_CNT = OFF_UF + (size_t)F_ * D_ * 2;

DI float bf2f(unsigned v) { return __uint_as_float(v << 16); }
DI float bflo(unsigned v) { return __uint_as_float(v << 16); }
DI float bfhi(unsigned v) { return __uint_as_float(v & 0xffff0000u); }
DI unsigned pack2(float a, float b) {
  typedef __attribute__((ext_vector_type(2))) __bf16 bf2;
  typedef __attribute__((ext_vector_type(2))) float f2;
  f2 v = {a, b};
  bf2 r = __builtin_convertvector(v, bf2);
  return __builtin_bit_cast(unsigned, r);
}
DI float bfr(float a) { return bflo(pack2(a, 0.f)); }
DI bf16x8 asbf(u32x4 v) { return __builtin_bit_cast(bf16x8, v); }

DI void glds16(const void* g, void* l) {
  __builtin_amdgcn_global_load_lds((const unsigned*)g, (unsigned*)l, 16, 0, 0);
}
DI void touch(const void* g, unsigned lds_dummy) {
  unsigned keep;
  asm volatile("s_mov_b32 %0, m0\n\ts_mov_b32 m0, %2\n\ts_nop 0\n\tglobal_load_lds_dword %1, off\n\ts_mov_b32 m0, %0"
               : "=&s"(keep) : "v"(g), "s"(lds_dummy) : "memory");
}
DI void store_sc1(void* g, u32x4 v) {
  asm volatile("global_store_dwordx4 %0, %1, off sc1\n\ts_nop 1" :: "v"(g), "v"(v) : "memory");
}
constexpr int NCNT_A = 16;
constexpr int CNT_STRIDE = 32;
DI void signal_counter(unsigned* c) {
  __hip_atomic_fetch_add(c, 1u, __ATOMIC_RELAXED, __HIP_MEMORY_SCOPE_AGENT);
}
DI void wait_counters(unsigned* c, int n, unsigned target, int l) {
  unsigned* p = c + (l < n ? l : 0) * CNT_STRIDE;
  for (int spin = 0; spin < 4000000; ++spin) {
    const unsigned v = __hip_atomic_load(p, __ATOMIC_RELAXED, __HIP_MEMORY_SCOPE_AGENT);
    if (__all(v >= target)) break;
    __builtin_amdgcn_s_sleep(1);
  }
}
DI s16x4 tr_read(const char* p) {
  return __builtin_amdgcn_ds_read_tr16_b64_v4i16((__attribute__((address_space(3))) s16x4*)(p));
}
#define STAMP(k, i) do {} while (0)
DI bf16x8 cat(s16x4 lo, s16x4 hi) { return __builtin_shufflevector(lo, hi, 0, 1, 2, 3, 4, 5, 6, 7); }

__global__ __launch_bounds__(256) void k1_ln_qkv(const u16* __restrict__ x, const u16* __restrict__ wq,
                                                 const u16* __restrict__ wk, const u16* __restrict__ wv,
                                                 const u16* __restrict__ wo, u16* __restrict__ Q,
                                                 u16* __restrict__ Kf, u16* __restrict__ Vf,
                                                 u16* __restrict__ WoF, unsigned* __restrict__ cnt) {
  __shared__ __attribute__((aligned(16))) char smem[D_ * 128 + 256];
  const int tid = threadIdx.x, l = tid & 63;
  const int w = __builtin_amdgcn_readfirstlane(tid >> 6);
  const int bx = blockIdx.x, xcd = bx & 7, ii = bx >> 3;
  const int tb = xcd + 8 * (ii >> 3), nt = ii & 7;
  const int g = l >> 4, i16 = l & 15;

  if (bx >= 256) {
    if (bx == 256 && tid < NCNT_A + 7) cnt[tid * CNT_STRIDE] = 0u;
#pragma unroll
    for (int r = 0; r < 2; ++r) {
      const int fi = (bx - 256) * 4 + w + 256 * r;
      if (fi < 392) {
        const int t = fi / 14, s = fi % 14;
        const u16* src = wo + (32 * s + 8 * g) * D_ + 16 * t + i16;
        u32x4 v;
#pragma unroll
        for (int j = 0; j < 4; ++j) v[j] = (unsigned)src[(2 * j) * D_] | ((unsigned)src[(2 * j + 1) * D_] << 16);
        const int dst = ((t / 7) * 2 + s / 7) * 49 + (s % 7) * 7 + t % 7;
        *(u32x4*)(WoF + ((size_t)dst * 64 + l) * 8) = v;
      }
    }
    return;
  }

  STAMP(0, 0);
  if (nt < 7) {
#pragma unroll
    for (int it = 0; it < 14; ++it) {
      const int piece = it * 4 + w;
      const int r = piece * 8 + (l >> 3);
      const int fr = ((r >> 1) & 1) | (((r >> 3) & 1) << 1);
      const int c = ((l & 7) >> 1) ^ fr;
      glds16(wq + r * D_ + nt * 64 + (2 * c + (l & 1)) * 8, smem + piece * 1024);
    }
  } else {
    const int col = (tid & 31) * 2, c16 = col & 31;
    const bool valid = c16 < DH_;
    const u16* wsrc = (col < 32 ? wk : wv) + (valid ? c16 : 0) + (tid >> 5) * DH_;
    unsigned v[56];
#pragma unroll
    for (int it = 0; it < 56; ++it) v[it] = *(const unsigned*)(wsrc + it * 8 * DH_);
#pragma unroll
    for (int it = 0; it < 56; ++it) {
      const int r = (tid >> 5) + 8 * it;
      const int fr = ((r >> 1) & 1) | (((r >> 3) & 1) << 1);
      *(unsigned*)(smem + r * 128 + (((col >> 4) ^ fr) * 32) + (col & 15) * 2) = valid ? v[it] : 0u;
    }
  }

  const int tok = tb * 64 + w * 16 + i16;
  u32x4 xf[14];
#pragma unroll
  for (int s = 0; s < 14; ++s) xf[s] = *(const u32x4*)(x + tok * D_ + 32 * s + 8 * g);
  const u32x4 ones = {0x3F803F80u, 0x3F803F80u, 0x3F803F80u, 0x3F803F80u};
  f32x4 accS = {0.f, 0.f, 0.f, 0.f}, accG = {0.f, 0.f, 0.f, 0.f};
#pragma unroll
  for (int s = 0; s < 14; ++s) {
    accS = MFMA16(asbf(ones), asbf(xf[s]), accS);
    accG = MFMA16(asbf(xf[s]), asbf(xf[s]), accG);
  }

  STAMP(0, 1);
  asm volatile("s_waitcnt vmcnt(0)" ::: "memory");
  __syncthreads();
  STAMP(0, 2);

  const int q4 = i16 >> 2, p4 = l & 3;
  const int fl = ((q4 >> 1) & 1) | ((g & 1) << 1);
  unsigned trb[4];
#pragma unroll
  for (int t = 0; t < 4; ++t) trb[t] = (unsigned)(size_t)smem + (8 * g + q4) * 128 + p4 * 8 + ((t ^ fl) * 32);
  f32x4 acc[4], accC = {0.f, 0.f, 0.f, 0.f};
#pragma unroll
  for (int t = 0; t < 4; ++t) acc[t] = (f32x4){0.f, 0.f, 0.f, 0.f};
  u32x2 wr[3][8];
#define K1_RD7(S) asm volatile( \
          "ds_read_b64_tr_b16 %0, %7 offset:%c11\n\t" \
          "ds_read_b64_tr_b16 %1, %7 offset:%c11+512\n\t" \
          "ds_read_b64_tr_b16 %2, %8 offset:%c11\n\t" \
          "ds_read_b64_tr_b16 %3, %8 offset:%c11+512\n\t" \
          "ds_read_b64_tr_b16 %4, %9 offset:%c11\n\t" \
          "ds_read_b64_tr_b16 %5, %9 offset:%c11+512\n\t" \
          "ds_read_b64_tr_b16 %6, %10 offset:%c11" \
          : "=&v"(wr[(S) % 3][0]), "=&v"(wr[(S) % 3][1]), "=&v"(wr[(S) % 3][2]), "=&v"(wr[(S) % 3][3]), \
            "=&v"(wr[(S) % 3][4]), "=&v"(wr[(S) % 3][5]), "=&v"(wr[(S) % 3][6]) \
          : "v"(trb[0]), "v"(trb[1]), "v"(trb[2]), "v"(trb[3]), "i"((S) * 4096) : "memory")
#define K1_RD8(S) asm volatile("ds_read_b64_tr_b16 %0, %1 offset:%c2" : "=&v"(wr[(S) % 3][7]) : "v"(trb[3]), "i"((S) * 4096 + 512) : "memory")
#define K1_WAIT(S, N) asm volatile("s_waitcnt lgkmcnt(%c8)" \
          : "+v"(wr[(S) % 3][0]), "+v"(wr[(S) % 3][1]), "+v"(wr[(S) % 3][2]), "+v"(wr[(S) % 3][3]), \
            "+v"(wr[(S) % 3][4]), "+v"(wr[(S) % 3][5]), "+v"(wr[(S) % 3][6]), "+v"(wr[(S) % 3][7]) : "i"(N) : "memory")
#define K1_WF(S, t) asbf((u32x4){wr[(S) % 3][2 * (t)][0], wr[(S) % 3][2 * (t)][1], wr[(S) % 3][2 * (t) + 1][0], wr[(S) % 3][2 * (t) + 1][1]})
#define K1_LOOP(KV) do { \
    _Pragma("unroll") for (int s = 0; s < 14; ++s) { \
      if (s < 12) { K1_RD7(s + 2); K1_WAIT(s, 15); K1_RD8(s + 2); } \
      else if (s == 12) { K1_WAIT(s, 8); } \
      else { K1_WAIT(s, 0); } \
      acc[0] = MFMA16(K1_WF(s, 0), asbf(xf[s]), acc[0]); \
      acc[1] = MFMA16(K1_WF(s, 1), asbf(xf[s]), acc[1]); \
      if (!(KV)) { \
        acc[2] = MFMA16(K1_WF(s, 2), asbf(xf[s]), acc[2]);        \
        acc[3] = MFMA16(K1_WF(s, 3), asbf(xf[s]), acc[3]); \
      } else { \
        acc[2] = MFMA16(asbf(xf[s]), K1_WF(s, 2), acc[2]);        \
        acc[3] = MFMA16(asbf(xf[s]), K1_WF(s, 3), acc[3]); \
      } \
    } } while (0)
  K1_RD7(0); K1_RD8(0); K1_RD7(1); K1_RD8(1);
  const bool kv = nt == 7;
  if (!kv) K1_LOOP(false); else K1_LOOP(true);
  {
    const unsigned cb = (unsigned)(size_t)smem + (8 * g + q4) * 128 + p4 * 8 + ((w ^ fl) * 32);
    u32x2 cr[14][2];
#pragma unroll
    for (int hs = 0; hs < 2; ++hs) {
#define CS_(i) "ds_read_b64_tr_b16 %" #i ", %14 offset:%c15+"
      asm volatile(
          "ds_read_b64_tr_b16 %0, %14 offset:%c15\n\t"       "ds_read_b64_tr_b16 %1, %14 offset:%c15+512\n\t"
          "ds_read_b64_tr_b16 %2, %14 offset:%c15+4096\n\t"  "ds_read_b64_tr_b16 %3, %14 offset:%c15+4608\n\t"
          "ds_read_b64_tr_b16 %4, %14 offset:%c15+8192\n\t"  "ds_read_b64_tr_b16 %5, %14 offset:%c15+8704\n\t"
          "ds_read_b64_tr_b16 %6, %14 offset:%c15+12288\n\t" "ds_read_b64_tr_b16 %7, %14 offset:%c15+12800\n\t"
          "ds_read_b64_tr_b16 %8, %14 offset:%c15+16384\n\t" "ds_read_b64_tr_b16 %9, %14 offset:%c15+16896\n\t"
          "ds_read_b64_tr_b16 %10, %14 offset:%c15+20480\n\t" "ds_read_b64_tr_b16 %11, %14 offset:%c15+20992\n\t"
          "ds_read_b64_tr_b16 %12, %14 offset:%c15+24576\n\t" "ds_read_b64_tr_b16 %13, %14 offset:%c15+25088\n\t"
          "s_waitcnt lgkmcnt(0)"
          : "=&v"(cr[7 * hs][0]), "=&v"(cr[7 * hs][1]), "=&v"(cr[7 * hs + 1][0]), "=&v"(cr[7 * hs + 1][1]),
            "=&v"(cr[7 * hs + 2][0]), "=&v"(cr[7 * hs + 2][1]), "=&v"(cr[7 * hs + 3][0]), "=&v"(cr[7 * hs + 3][1]),
            "=&v"(cr[7 * hs + 4][0]), "=&v"(cr[7 * hs + 4][1]), "=&v"(cr[7 * hs + 5][0]), "=&v"(cr[7 * hs + 5][1]),
            "=&v"(cr[7 * hs + 6][0]), "=&v"(cr[7 * hs + 6][1])
          : "v"(cb), "i"(hs * 7 * 4096) : "memory");
#undef CS_
    }
#pragma unroll
    for (int s = 0; s < 14; ++s)
      accC = MFMA16(asbf(ones), asbf((u32x4){cr[s][0][0], cr[s][0][1], cr[s][1][0], cr[s][1][1]}), accC);
  }
  __syncthreads();
  float* csb = (float*)smem;
  if (g == 0) csb[16 * w + i16] = accC[0];
  float mb, rs;
  {
    const int r3 = l & 3;
    const float cand = r3 == 0 ? accG[0] : r3 == 1 ? accG[1] : r3 == 2 ? accG[2] : accG[3];
    const float g2 = __shfl(cand, i16 + 16 * (i16 >> 2));
    const float mean = accS[0] * (1.0f / D_);
    const float var = fmaxf(g2 * (1.0f / D_) - mean * mean, 0.f);
    mb = bfr(mean);
    rs = __builtin_amdgcn_rcpf(bfr(__builtin_amdgcn_sqrtf(bfr(bfr(var) + 1e-5f))));
  }
  __syncthreads();
  if (!kv) {
    const float qs = rs * (0.18898223650461362f * 1.4426950408889634f);
#pragma unroll
    for (int t = 0; t < 4; ++t) {
      const int n = nt * 64 + 16 * t + 4 * g;
      const int head = n / DH_, d = n - head * DH_;
      const f32x4 cs = *(const f32x4*)(csb + 16 * t + 4 * g);
      u32x2 o;
      o[0] = pack2((acc[t][0] - mb * cs[0]) * qs, (acc[t][1] - mb * cs[1]) * qs);
      o[1] = pack2((acc[t][2] - mb * cs[2]) * qs, (acc[t][3] - mb * cs[3]) * qs);
      *(u32x2*)(Q + ((size_t)tok * H_ + head) * 32 + d) = o;
    }
  } else {
#pragma unroll
    for (int t = 0; t < 2; ++t) {
      const int d = 16 * t + 4 * g;
      const f32x4 cs = *(const f32x4*)(csb + d);
      u32x2 o;
      o[0] = pack2((acc[t][0] - mb * cs[0]) * rs, (acc[t][1] - mb * cs[1]) * rs);
      o[1] = pack2((acc[t][2] - mb * cs[2]) * rs, (acc[t][3] - mb * cs[3]) * rs);
      if (d == 28) { o[0] = 0x3F803F80u; o[1] = 0x00003F80u; }
      const int blk = tok >> 5, hh = (d & 15) >> 3, j = d & 7;
      *(u32x2*)(Kf + ((size_t)((blk * 2 + t) * 64 + (tok & 31) + 32 * hh)) * 8 + j) = o;
    }
    float mbk[4], rsk[4];
#pragma unroll
    for (int r = 0; r < 4; ++r) { mbk[r] = __shfl(mb, 4 * g + r); rsk[r] = __shfl(rs, 4 * g + r); }
#pragma unroll
    for (int t = 2; t < 4; ++t) {
      const int d = 16 * (t - 2) + i16;
      const float cs = csb[16 * t + i16];
      u32x2 o;
      o[0] = pack2((acc[t][0] - mbk[0] * cs) * rsk[0], (acc[t][1] - mbk[1] * cs) * rsk[1]);
      o[1] = pack2((acc[t][2] - mbk[2] * cs) * rsk[2], (acc[t][3] - mbk[3] * cs) * rsk[3]);
      if (d == 28) { o[0] = 0x3F803F80u; o[1] = 0x3F803F80u; }
      const int blk = tb * 2 + (w >> 1), s = w & 1;
      *(u32x2*)(Vf + ((size_t)((blk * 2 + s) * 64 + d + 32 * (g & 1))) * 8 + 4 * (g >> 1)) = o;
    }
  }
  STAMP(0, 3);
}

constexpr int PART_ROW = 144;
constexpr int PART_W = 128 * PART_ROW;
constexpr int K2_OBUF = 8 * PART_W;
constexpr int K2_DUMMY = K2_OBUF + 8 * D_ * 2;
constexpr int K2_SMEM = K2_DUMMY + 256;

DI float wave_sum(float v) {
  v += __builtin_bit_cast(float, __builtin_amdgcn_update_dpp(0, __builtin_bit_cast(int, v), 0x111, 0xf, 0xf, false));
  v += __builtin_bit_cast(float, __builtin_amdgcn_update_dpp(0, __builtin_bit_cast(int, v), 0x112, 0xf, 0xf, false));
  v += __builtin_bit_cast(float, __builtin_amdgcn_update_dpp(0, __builtin_bit_cast(int, v), 0x114, 0xf, 0xf, false));
  v += __builtin_bit_cast(float, __builtin_amdgcn_update_dpp(0, __builtin_bit_cast(int, v), 0x118, 0xf, 0xf, false));
  v += __builtin_bit_cast(float, __builtin_amdgcn_update_dpp(0, __builtin_bit_cast(int, v), 0x142, 0xa, 0xf, false));
  v += __builtin_bit_cast(float, __builtin_amdgcn_update_dpp(0, __builtin_bit_cast(int, v), 0x143, 0xc, 0xf, false));
  return __builtin_bit_cast(float, __builtin_amdgcn_readlane(__builtin_bit_cast(int, v), 63));
}

DI void attn_phase(char* smem, const u16* __restrict__ Q, const u16* __restrict__ Kf,
                   const u16* __restrict__ Vf, const u16* __restrict__ WoF, const u16* __restrict__ x,
                   u16* Hout, u16* HnF, const u16* __restrict__ w1, const u16* __restrict__ w2) {
  const int tid = threadIdx.x, l = tid & 63, h = l >> 5;
  const int w = __builtin_amdgcn_readfirstlane(tid >> 6);
  const int t0 = blockIdx.x * 8;

  STAMP(1, 0);
  if (w < 4) __builtin_amdgcn_s_setprio(2);
  const u32x4* kbase = (const u32x4*)Kf + (size_t)w * 8 * 128 + l;
  const u32x4* vbase = (const u32x4*)Vf + (size_t)w * 8 * 128 + l;
  u32x4 qf[4][2];
#pragma unroll
  for (int b = 0; b < 4; ++b)
#pragma unroll
    for (int s = 0; s < 2; ++s)
      qf[b][s] = *(const u32x4*)(Q + ((size_t)(t0 * H_ + 32 * b + (l & 31))) * 32 + 16 * s + 8 * h);

  const int role = (w + (blockIdx.x >> 3)) & 7;
  const int kh = role & 1, nq = role >> 1;
  const u32x4* wbase = (const u32x4*)WoF + (size_t)(role * 49) * 64 + l;
  constexpr int WO_EARLY = 0;
  u32x4 woe[WO_EARLY + 1];

  f32x16 zero16;
#pragma unroll
  for (int i = 0; i < 16; ++i) zero16[i] = 0.f;

  float mval[4];
  f32x16 o[4];
  int nmax = 1;
#pragma unroll
  for (int e = 0; e < WO_EARLY; ++e) woe[e] = wbase[e * 64];
  for (;;) {
    asm volatile("" : "+v"(kbase), "+v"(vbase));
    u32x4 k0n = kbase[0], k1n = kbase[64], v0n = vbase[0], v1n = vbase[64];
    u32x4 k0m = kbase[128], k1m = kbase[192], v0m = vbase[128], v1m = vbase[192];
    float mx[4];
#pragma unroll
    for (int b = 0; b < 4; ++b) {
      if (h) { qf[b][1][2] = 0u; qf[b][1][3] = 0u; }
      f32x16 s = MFMA32(asbf(k0n), asbf(qf[b][0]), zero16);
      s = MFMA32(asbf(k1n), asbf(qf[b][1]), s);
      float m = s[0];
#pragma unroll
      for (int i = 1; i < 16; ++i) m = fmaxf(m, s[i]);
      mx[b] = m;
    }
    for (int kb = 1; kb < nmax; ++kb) {
      const u32x4 k0 = kbase[kb * 128], k1 = kbase[kb * 128 + 64];
#pragma unroll
      for (int b = 0; b < 4; ++b) {
        f32x16 s = MFMA32(asbf(k0), asbf(qf[b][0]), zero16);
        s = MFMA32(asbf(k1), asbf(qf[b][1]), s);
#pragma unroll
        for (int i = 0; i < 16; ++i) mx[b] = fmaxf(mx[b], s[i]);
      }
    }
#pragma unroll
    for (int b = 0; b < 4; ++b) {
      const auto sw = __builtin_amdgcn_permlane32_swap(__float_as_uint(mx[b]), __float_as_uint(mx[b]), false, false);
      const float m = fmaxf(__uint_as_float(sw[0]), __uint_as_float(sw[1]));
      mval[b] = m;
      const float nm = -m;
      const unsigned hi = __float_as_uint(nm) & 0xffff0000u;
      const float r1 = nm - __uint_as_float(hi);
      const unsigned mid = __float_as_uint(r1) & 0xffff0000u;
      const float r2 = r1 - __uint_as_float(mid);
      const unsigned lo = __float_as_uint(r2) & 0xffff0000u;
      if (h) { qf[b][1][2] = (hi >> 16) | mid; qf[b][1][3] = lo >> 16; }
      o[b] = zero16;
    }
    STAMP(1, 1);
#define SB() __builtin_amdgcn_sched_barrier(0)
#define EXP4(S, i) do { S[i] = __builtin_amdgcn_exp2f(S[i]); S[i + 1] = __builtin_amdgcn_exp2f(S[i + 1]); \
                        S[i + 2] = __builtin_amdgcn_exp2f(S[i + 2]); S[i + 3] = __builtin_amdgcn_exp2f(S[i + 3]); } while (0)
#define STEP(SC, SN, B, BN, BP, KN0, KN1, V0, VP1) do { \
      u32x4 p0; \
      o[BP] = MFMA32(asbf(VP1), asbf(pp), o[BP]); SB(); \
      EXP4(SC, 0); SB(); \
      SN = MFMA32(asbf(KN0), asbf(qf[BN][0]), zero16); SB(); \
      EXP4(SC, 4); \
      p0[0] = pack2(SC[0], SC[1]); p0[1] = pack2(SC[2], SC[3]); p0[2] = pack2(SC[4], SC[5]); p0[3] = pack2(SC[6], SC[7]); SB(); \
      asm volatile("" :: "v"(pp)); \
      SN = MFMA32(asbf(KN1), asbf(qf[BN][1]), SN); SB(); \
      EXP4(SC, 8); SB(); \
      o[B] = MFMA32(asbf(V0), asbf(p0), o[B]); SB(); \
      EXP4(SC, 12); \
      pp[0] = pack2(SC[8], SC[9]); pp[1] = pack2(SC[10], SC[11]); pp[2] = pack2(SC[12], SC[13]); pp[3] = pack2(SC[14], SC[15]); SB(); \
      asm volatile("" :: "v"(p0)); \
    } while (0)
    {
      u32x4 kA0 = k0n, kA1 = k1n, vA0 = v0n, vA1 = v1n;
      u32x4 kB0 = k0m, kB1 = k1m, vB0 = v0m, vB1 = v1m;
      u32x4 kC0, kC1, vC0, vC1 = v1n;
      u32x4 pp = {0u, 0u, 0u, 0u};
      f32x16 sa, sb;
      sa = MFMA32(asbf(kA0), asbf(qf[0][0]), zero16);
      sa = MFMA32(asbf(kA1), asbf(qf[0][1]), sa);
#define BLOCK(KB, X, Y, Z, SA, SB_) do { \
        const int kn_ = ((KB) + 2) & 7; \
        k##Z##0 = kbase[kn_ * 128]; k##Z##1 = kbase[kn_ * 128 + 64]; v##Z##0 = vbase[kn_ * 128]; \
        SB(); \
        STEP(SA, SB_, 0, 1, 3, k##X##0, k##X##1, v##X##0, v##Z##1); \
        v##Z##1 = vbase[kn_ * 128 + 64]; \
        SB(); \
        STEP(SB_, SA, 1, 2, 0, k##X##0, k##X##1, v##X##0, v##X##1); \
        STEP(SA, SB_, 2, 3, 1, k##X##0, k##X##1, v##X##0, v##X##1); \
        STEP(SB_, SA, 3, 0, 2, k##Y##0, k##Y##1, v##X##0, v##X##1); \
      } while (0)
#pragma unroll 1
      for (int kb = 0; kb < 6; kb += 3) {
        BLOCK(kb, A, B, C, sa, sb);
        if (kb == 0 || w == 0) {
          const int job = kb == 0 ? w : 8;
          const int xcd = blockIdx.x & 7, bi = blockIdx.x >> 3;
          const u16* pf;
          if (job < 5) {
            const int idx = job * 64 + l, row = idx / 5, ln = idx - row * 5;
            int col = ((xcd * 49) >> 1) / 7 * 64 + ln * 64;
            col = col < F_ - 64 ? col : F_ - 64;
            pf = w1 + (size_t)(64 * bi + row) * F_ + col;
          } else {
            int idx = (job - 5) * 64 + l;
            idx = idx < 224 ? idx : 223;
            pf = w2 + (size_t)(56 * bi + (idx >> 2)) * S_ + 256 * xcd + (idx & 3) * 64;
          }
          touch(pf, (unsigned)(size_t)(smem + K2_DUMMY));
        }
        BLOCK(kb + 1, B, C, A, sa, sb);
        BLOCK(kb + 2, C, A, B, sa, sb);
      }
      BLOCK(6, A, B, C, sa, sb);
      BLOCK(7, B, C, A, sa, sb);
      o[3] = MFMA32(asbf(vB1), asbf(pp), o[3]);
    }
    bool ok = true;
#pragma unroll
    for (int b = 0; b < 4; ++b) ok = ok && (h == 0 || o[b][12] < 1.8446744e19f);
    if (nmax == 8 || __all(ok)) break;
    nmax = 8;
  }
  STAMP(1, 2);
  {
    char* pw = smem + w * PART_W;
#pragma unroll
    for (int b = 0; b < 4; ++b) {
      char* pr = pw + (32 * b + (l & 31)) * PART_ROW + 16 * h;
#pragma unroll
      for (int gq = 0; gq < 4; ++gq)
        *(f32x4*)(pr + 32 * gq) = (f32x4){o[b][4 * gq], o[b][4 * gq + 1], o[b][4 * gq + 2], o[b][4 * gq + 3]};
      if (h == 0) *(float*)(pr + 128) = mval[b];
    }
  }
  u32x4 xr;
  __builtin_amdgcn_s_setprio(0);
#define LOAD_WOF(S0, S1) do { \
    _Pragma("unroll") for (int s = S0; s < S1; ++s) \
      _Pragma("unroll") for (int i = 0; i < 7; ++i) \
        wof[s][i] = (s * 7 + i < WO_EARLY) ? woe[s * 7 + i] : wbase[(s * 7 + i) * 64]; \
  } while (0)
#define LOAD_XR() do { xr = *(const u32x4*)(x + (t0 + w) * D_ + 8 * (l < 56 ? l : 55)); } while (0)
#define WO_GEMM() do { \
    const int g = l >> 4; \
    f32x4 acc[7]; \
    _Pragma("unroll") for (int i = 0; i < 7; ++i) acc[i] = (f32x4){0.f, 0.f, 0.f, 0.f}; \
    _Pragma("unroll") for (int s = 0; s < 7; ++s) { \
      const u32x4 of = *(const u32x4*)(smem + K2_OBUF + (l & 7) * (D_ * 2) + 64 * (7 * kh + s) + 16 * g); \
      _Pragma("unroll") for (int i = 0; i < 7; ++i) acc[i] = MFMA16(asbf(wof[s][i]), asbf(of), acc[i]); \
    } \
    if ((l & 15) < 8) { \
      _Pragma("unroll") for (int i = 0; i < 7; ++i) \
        *(f32x4*)(smem + ((kh * 8 + (l & 15)) * D_ + 16 * (7 * nq + i) + 4 * g) * 4) = acc[i]; \
    } \
  } while (0)
  if (w < 4) {
    u32x4 wof[7][7];
    LOAD_WOF(0, 6);
    __builtin_amdgcn_sched_barrier(0);
    STAMP(1, 3);
    __syncthreads();
    STAMP(1, 4);
#pragma unroll 1
    for (int rep = 0; rep < 2; ++rep) {
      const int item = rep * 256 + tid;
      const int row = item >> 2, qd = item & 3;
      const char* pr0 = smem + row * PART_ROW;
      float M = -3.0e38f;
#pragma unroll
      for (int ww = 0; ww < 8; ++ww) M = fmaxf(M, *(const float*)(pr0 + ww * PART_W + 128));
      float lsum = 0.f;
      f32x4 a0 = {0.f, 0.f, 0.f, 0.f}, a1 = {0.f, 0.f, 0.f, 0.f};
#pragma unroll
      for (int ww = 0; ww < 8; ++ww) {
        const char* pr = pr0 + ww * PART_W;
        const float e = __builtin_amdgcn_exp2f(*(const float*)(pr + 128) - M);
        lsum += e * *(const float*)(pr + 112);
        a0 += e * *(const f32x4*)(pr + 32 * qd);
        a1 += e * *(const f32x4*)(pr + 32 * qd + 16);
      }
      const float inv = __builtin_amdgcn_rcpf(lsum);
      char* ob = smem + K2_OBUF + (row >> 4) * (D_ * 2) + (row & 15) * (DH_ * 2) + 16 * qd;
      u32x2 o0, o1;
      o0[0] = pack2(a0[0] * inv, a0[1] * inv); o0[1] = pack2(a0[2] * inv, a0[3] * inv);
      o1[0] = pack2(a1[0] * inv, a1[1] * inv); o1[1] = pack2(a1[2] * inv, a1[3] * inv);
      *(u32x2*)ob = o0;
      if (qd < 3) *(u32x2*)(ob + 8) = o1;
    }
    LOAD_WOF(6, 7);
    LOAD_XR();
    __builtin_amdgcn_sched_barrier(0);
    STAMP(1, 5);
    __syncthreads();
    STAMP(1, 6);
    WO_GEMM();
  } else {
    STAMP(1, 3);
    __syncthreads();
    STAMP(1, 4);
    u32x4 wof[7][7];
    LOAD_WOF(0, 7);
    LOAD_XR();
    __builtin_amdgcn_sched_barrier(0);
    STAMP(1, 5);
    __syncthreads();
    STAMP(1, 6);
    WO_GEMM();
  }
  STAMP(1, 7);
  __syncthreads();
  STAMP(1, 8);
  {
    const int lc = l < 56 ? l : 55;
    const float live = l < 56 ? 1.0f : 0.0f;
    const char* r0 = smem + (w * D_ + 8 * lc) * 4;
    const f32x4 a0 = *(const f32x4*)r0 + *(const f32x4*)(r0 + 8 * D_ * 4);
    const f32x4 a1 = *(const f32x4*)(r0 + 16) + *(const f32x4*)(r0 + 8 * D_ * 4 + 16);
    u32x4 hv;
    float v[8];
#pragma unroll
    for (int j = 0; j < 4; ++j) {
      const unsigned ab = j < 2 ? pack2(a0[2 * j], a0[2 * j + 1]) : pack2(a1[2 * j - 4], a1[2 * j - 3]);
      hv[j] = pack2(bflo(xr[j]) + bflo(ab), bfhi(xr[j]) + bfhi(ab));
      v[2 * j] = bflo(hv[j]); v[2 * j + 1] = bfhi(hv[j]);
    }
    if (l < 56) store_sc1(Hout + (t0 + w) * D_ + 8 * l, hv);
#pragma unroll
    for (int rep = 0; rep < 2; ++rep) {
      const float c = __builtin_bit_cast(float, __builtin_amdgcn_readfirstlane(__builtin_bit_cast(int, v[0])));
      float s1 = 0.f, s2 = 0.f;
#pragma unroll
      for (int i = 0; i < 8; ++i) { const float a = v[i] - c; s1 += a; s2 = fmaf(a, a, s2); }
      s1 = wave_sum(s1 * live) * (1.0f / D_);
      s2 = wave_sum(s2 * live) * (1.0f / D_);
      const float mean = c + s1;
      const float var = fmaxf(s2 - s1 * s1, 0.f);
      const float mb = bfr(mean);
      const float rs = __builtin_amdgcn_rcpf(bfr(__builtin_amdgcn_sqrtf(bfr(bfr(var) + 1e-5f))));
      const float nb = -mb * rs;
#pragma unroll
      for (int j = 0; j < 4; ++j) {
        hv[j] = pack2(fmaf(v[2 * j], rs, nb), fmaf(v[2 * j + 1], rs, nb));
        v[2 * j] = bflo(hv[j]); v[2 * j + 1] = bfhi(hv[j]);
      }
    }
    if (l < 56) *(u32x4*)(smem + K2_OBUF + (w * D_ + 8 * l) * 2) = hv;
  }
  STAMP(1, 9);
  __syncthreads();
  STAMP(1, 10);
  if (tid < D_) {
    const u16* hb = (const u16*)(smem + K2_OBUF);
    u32x4 v;
#pragma unroll
    for (int j = 0; j < 4; ++j)
      v[j] = (unsigned)hb[(2 * j) * D_ + tid] | ((unsigned)hb[(2 * j + 1) * D_ + tid] << 16);
    const int lane = ((t0 >> 3) & 3) * 16 + (tid & 15);
    store_sc1(HnF + ((size_t)((((tid >> 6) * 64 + (t0 >> 5)) * 4 + ((tid >> 4) & 3)) * 64 + lane)) * 8, v);
  }
  STAMP(1, 11);
}

constexpr int G_SMEM = 8 * 16384;
constexpr int GDEPTH = 4;

template <int MODE>
DI void gemm_phase(char* smem, int dt, int nt, const u16* __restrict__ W, const u16* act,
                   const u16* __restrict__ bias, const u16* Hres, u16* out,
                   unsigned* wait_cnt, int wait_n, unsigned wait_target) {
  constexpr int LDW = MODE == 0 ? F_ : S_;
  constexpr int NKS = MODE == 0 ? S_ / 32 : F_ / 32;
  constexpr int KPW = NKS / 8;
  const int tid = threadIdx.x, l = tid & 63;
  const int w = __builtin_amdgcn_readfirstlane(tid >> 6);
  char* ring = smem + w * (GDEPTH * 4096);
  const int g = l >> 4, i16 = l & 15, q4 = i16 >> 2, p4 = l & 3;
  const u16* asrc = act + ((size_t)dt * NKS + w * KPW) * 2048 + l * 8;
  const int fsrc = (l >> 4) & 1;
  const u16* wsrc = W + (size_t)(w * KPW * 32 + (l >> 3)) * LDW + 64 * nt + (l & 1) * 8;
  const int c0 = (l & 7) >> 1;
  const int ecol = tid >> 3, er8 = (tid & 7) * 8;
  const unsigned ring_a = (unsigned)(size_t)ring;

  u32x4 ar[GDEPTH][4];
  auto issueW = [&](int i) {
#pragma unroll
    for (int j = 0; j < 4; ++j) {
      const int c = c0 ^ (fsrc | ((j & 1) << 1));
      unsigned keep;
      asm volatile("s_mov_b32 %0, m0\n\ts_mov_b32 m0, %2\n\ts_nop 0\n\tglobal_load_lds_dwordx4 %1, off\n\ts_mov_b32 m0, %0"
                   : "=&s"(keep) : "v"(wsrc + (size_t)(i * 32 + 8 * j) * LDW + c * 16),
                     "s"(ring_a + (i % GDEPTH) * 4096 + j * 1024) : "memory");
    }
  };
  auto issueA = [&](int i) {
#pragma unroll
    for (int j = 0; j < 4; ++j)
      asm volatile("global_load_dwordx4 %0, %1, off sc1" : "=v"(ar[i % GDEPTH][j]) : "v"(asrc + (size_t)(i * 4 + j) * 512) : "memory");
  };

  STAMP(2 + MODE, 0);
#pragma unroll
  for (int i = 0; i < GDEPTH; ++i) issueW(i);
  if (w == 0) wait_counters(wait_cnt, wait_n, wait_target, l);
  asm volatile("s_waitcnt lgkmcnt(0)" ::: "memory");
  __builtin_amdgcn_s_barrier();
  asm volatile("" ::: "memory");
  STAMP(2 + MODE, 1);
#pragma unroll
  for (int i = 0; i < GDEPTH; ++i) issueA(i);

  f32x4 acc[4][4];
#pragma unroll
  for (int a = 0; a < 4; ++a)
#pragma unroll
    for (int b = 0; b < 4; ++b) acc[a][b] = (f32x4){0.f, 0.f, 0.f, 0.f};

  const int fl = ((q4 >> 1) & 1) | ((g & 1) << 1);
  unsigned trb[4];
#pragma unroll
  for (int t = 0; t < 4; ++t) trb[t] = ring_a + (8 * g + q4) * 128 + p4 * 8 + ((t ^ fl) * 32);

  u32x4 ep;
  unsigned epb = 0;
#pragma unroll
  for (int i = 0; i < KPW; ++i) {
    const int full = (KPW - 1 - i) < (GDEPTH - 1) ? (KPW - 1 - i) : (GDEPTH - 1);
    const int early = i < GDEPTH ? (GDEPTH - 1 - i < full ? GDEPTH - 1 - i : full) : 0;
    const int extra = (i == KPW - 1) ? (MODE == 0 ? 1 : 2) : 0;
    const int pend = early * 4 + (full - early) * 8 + extra;
    const int so = (i % GDEPTH) * 4096;
    if (i == KPW - 1) {
      if (MODE == 0) {
        asm volatile("global_load_dwordx4 %0, %1, off" : "=v"(ep) : "v"(bias + 64 * nt + er8) : "memory");
      } else {
        asm volatile("global_load_dwordx4 %0, %1, off sc1" : "=v"(ep) : "v"(Hres + (size_t)(64 * nt + ecol) * D_ + 64 * dt + er8) : "memory");
        asm volatile("global_load_ushort %0, %1, off" : "=v"(epb) : "v"(bias + 64 * nt + ecol) : "memory");
      }
    }
    bf16x8 wf[4];
    u32x4 a0 = ar[i % GDEPTH][0], a1 = ar[i % GDEPTH][1], a2 = ar[i % GDEPTH][2], a3 = ar[i % GDEPTH][3];
    {
      u32x2 w0l, w0h, w1l, w1h, w2l, w2h, w3l, w3h;
      asm volatile(
          "s_waitcnt vmcnt(%c18)\n\t"
          "ds_read_b64_tr_b16 %0, %12 offset:%c16\n\t"
          "ds_read_b64_tr_b16 %1, %12 offset:%c17\n\t"
          "ds_read_b64_tr_b16 %2, %13 offset:%c16\n\t"
          "ds_read_b64_tr_b16 %3, %13 offset:%c17\n\t"
          "ds_read_b64_tr_b16 %4, %14 offset:%c16\n\t"
          "ds_read_b64_tr_b16 %5, %14 offset:%c17\n\t"
          "ds_read_b64_tr_b16 %6, %15 offset:%c16\n\t"
          "ds_read_b64_tr_b16 %7, %15 offset:%c17\n\t"
          "s_waitcnt lgkmcnt(0)"
          : "=&v"(w0l), "=&v"(w0h), "=&v"(w1l), "=&v"(w1h), "=&v"(w2l), "=&v"(w2h), "=&v"(w3l), "=&v"(w3h),
            "+v"(a0), "+v"(a1), "+v"(a2), "+v"(a3)
          : "v"(trb[0]), "v"(trb[1]), "v"(trb[2]), "v"(trb[3]), "i"(so), "i"(so + 512), "i"(pend)
          : "memory");
      wf[0] = asbf((u32x4){w0l[0], w0l[1], w0h[0], w0h[1]});
      wf[1] = asbf((u32x4){w1l[0], w1l[1], w1h[0], w1h[1]});
      wf[2] = asbf((u32x4){w2l[0], w2l[1], w2h[0], w2h[1]});
      wf[3] = asbf((u32x4){w3l[0], w3l[1], w3h[0], w3h[1]});
    }
    if (i + GDEPTH < KPW) { issueW(i + GDEPTH); issueA(i + GDEPTH); }
    const bf16x8 af[4] = {asbf(a0), asbf(a1), asbf(a2), asbf(a3)};
#pragma unroll
    for (int a = 0; a < 4; ++a)
#pragma unroll
      for (int b = 0; b < 4; ++b) {
        if (MODE == 0) acc[a][b] = MFMA16(wf[a], af[b], acc[a][b]);
        else           acc[a][b] = MFMA16(af[a], wf[b], acc[a][b]);
      }
  }
  STAMP(2 + MODE, 2);
  {
#pragma unroll
    for (int a = 0; a < 4; ++a)
#pragma unroll
      for (int b = 0; b < 4; ++b)
        *(f32x4*)(ring + (16 * b + i16) * 256 + (((4 * a + g) ^ i16) * 16)) = acc[a][b];
  }
  asm volatile("s_waitcnt vmcnt(0)" : "+v"(ep), "+v"(epb) :: "memory");
  STAMP(2 + MODE, 3);
  __syncthreads();
  {
    f32x4 s0 = {0.f, 0.f, 0.f, 0.f}, s1 = {0.f, 0.f, 0.f, 0.f};
    const int ch = (tid & 7) * 2;
    const char* p0 = smem + ecol * 256 + ((ch ^ (ecol & 15)) * 16);
    const char* p1 = smem + ecol * 256 + (((ch + 1) ^ (ecol & 15)) * 16);
#pragma unroll
    for (int ww = 0; ww < 8; ++ww) {
      s0 += *(const f32x4*)(p0 + ww * 16384);
      s1 += *(const f32x4*)(p1 + ww * 16384);
    }
    float v[8] = {s0[0], s0[1], s0[2], s0[3], s1[0], s1[1], s1[2], s1[3]};
    u32x4 ov;
    if (MODE == 0) {
      const int d = 64 * dt + ecol, f = 64 * nt + er8;
#pragma unroll
      for (int j = 0; j < 8; ++j) {
        const float bb = (j & 1) ? bfhi(ep[j >> 1]) : bflo(ep[j >> 1]);
        const float t = bfr(bfr(v[j]) + bb);
        const float y = 0.7978845608028654f * (t + 0.044715f * t * t * t);
        v[j] = t * __builtin_amdgcn_rcpf(1.0f + __expf(-2.0f * y));
      }
#pragma unroll
      for (int j = 0; j < 4; ++j) ov[j] = pack2(v[2 * j], v[2 * j + 1]);
      const int lane = ((f & 31) >> 3) * 16 + (d & 15);
      store_sc1(out + ((size_t)(((d >> 6) * (F_ / 32) + (f >> 5)) * 4 + ((d >> 4) & 3)) * 64 + lane) * 8, ov);
    } else {
      const int s = 64 * nt + ecol, d = 64 * dt + er8;
      const float bb = bf2f(epb);
#pragma unroll
      for (int j = 0; j < 8; ++j) {
        const float hh = (j & 1) ? bfhi(ep[j >> 1]) : bflo(ep[j >> 1]);
        v[j] = hh + bfr(bfr(v[j]) + bb);
      }
#pragma unroll
      for (int j = 0; j < 4; ++j) ov[j] = pack2(v[2 * j], v[2 * j + 1]);
      *(u32x4*)(out + (size_t)s * D_ + d) = ov;
    }
  }
  STAMP(2 + MODE, 4);
}

__global__ __launch_bounds__(512, 2) void k_fused(const u16* __restrict__ Q, const u16* __restrict__ Kf,
                                                  const u16* __restrict__ Vf, const u16* __restrict__ WoF,
                                                  const u16* __restrict__ x, u16* Hb, u16* HnF, u16* UF,
                                                  const u16* __restrict__ w1, const u16* __restrict__ b1,
                                                  const u16* __restrict__ w2, const u16* __restrict__ b2,
                                                  u16* out, unsigned* cnt) {
  extern __shared__ __attribute__((aligned(16))) char smem[];
  const int tid = threadIdx.x;
  const int xcd = blockIdx.x & 7, bi = blockIdx.x >> 3;
  unsigned* cntA = cnt;
  unsigned* cntB = cnt + NCNT_A * CNT_STRIDE;

  attn_phase(smem, Q, Kf, Vf, WoF, x, Hb, HnF, w1, w2);
  asm volatile("s_waitcnt vmcnt(0)" ::: "memory");
  __syncthreads();
  if (tid == 0) signal_counter(cntA + (blockIdx.x & (NCNT_A - 1)) * CNT_STRIDE);

  {
    const int start = (xcd * 49) >> 1, cntx = ((xcd + 1) * 49 >> 1) - start;
    if (bi < cntx) {
      const int j = start + bi, nt = j / 7, dt = j - nt * 7;
      gemm_phase<0>(smem, dt, nt, w1, HnF, b1, nullptr, UF, cntA, NCNT_A, 256 / NCNT_A);
      asm volatile("s_waitcnt vmcnt(0)" ::: "memory");
      __syncthreads();
      if (tid == 0) signal_counter(cntB + dt * CNT_STRIDE);
    }
  }
  if (bi < 28) {
    const int j = xcd * 28 + bi, nt = j / 7, dt = j - nt * 7;
    gemm_phase<1>(smem, dt, nt, w2, UF, b2, Hb, out, cntB + dt * CNT_STRIDE, 1, 28);
  }
}

extern "C" void kernel_launch(void* const* d_in, const int* in_sizes, int n_in, void* d_out, int out_size,
                              void* d_ws, size_t ws_size, hipStream_t stream) {
  const u16* x  = (const u16*)d_in[0];
  const u16* wq = (const u16*)d_in[1];
  const u16* wk = (const u16*)d_in[2];
  const u16* wv = (const u16*)d_in[3];
  const u16* wo = (const u16*)d_in[4];
  const u16* w1 = (const u16*)d_in[6];
  const u16* b1 = (const u16*)d_in[7];
  const u16* w2 = (const u16*)d_in[8];
  const u16* b2 = (const u16*)d_in[9];
  char* ws = (char*)d_ws;
  u16* Q   = (u16*)(ws + OFF_Q);
  u16* Kf  = (u16*)(ws + OFF_KF);
  u16* Vf  = (u16*)(ws + OFF_VF);
  u16* WoF = (u16*)(ws + OFF_WOF);
  u16* Hb  = (u16*)(ws + OFF_H);
  u16* HnF = (u16*)(ws + OFF_HNF);
  u16* UF  = (u16*)(ws + OFF_UF);

  unsigned* cnt = (unsigned*)(ws + OFF_CNT);

  (void)hipFuncSetAttribute((const void*)k_fused, hipFuncAttributeMaxDynamicSharedMemorySize, K2_SMEM);

  k1_ln_qkv<<<320, 256, 0, stream>>>(x, wq, wk, wv, wo, Q, Kf, Vf, WoF, cnt);
  k_fused<<<256, 512, K2_SMEM, stream>>>(Q, Kf, Vf, WoF, x, Hb, HnF, UF, w1, b1, w2, b2, (u16*)d_out, cnt);
}
```

```cpp
#include <hip/hip_runtime.h>
#include <hip/hip_bf16.h>
#include <stdint.h>

typedef unsigned short u16;
typedef __attribute__((ext_vector_type(8))) short bf16x8;
typedef __attribute__((ext_vector_type(4))) short s16x4;
typedef __attribute__((ext_vector_type(4))) float f32x4;
typedef __attribute__((ext_vector_type(16))) float f32x16;
typedef __attribute__((ext_vector_type(4))) unsigned u32x4;
typedef __attribute__((ext_vector_type(2))) unsigned u32x2;

#define DI __device__ __forceinline__
#define MFMA32(a, b, c) __builtin_amdgcn_mfma_f32_32x32x16_bf16((a), (b), (c), 0, 0, 0)
#define MFMA16(a, b, c) __builtin_amdgcn_mfma_f32_16x16x32_bf16((a), (b), (c), 0, 0, 0)

constexpr int S_ = 2048, D_ = 448, H_ = 16, DH_ = 28, F_ = 1792;

constexpr size_t OFF_Q   = 0;
constexpr size_t OFF_KF  = OFF_Q + (size_t)S_ * H_ * 32 * 2;
constexpr size_t OFF_VF  = OFF_KF + (size_t)S_ * 32 * 2;
constexpr size_t OFF_WOF = OFF_VF + (size_t)S_ * 32 * 2;
constexpr size_t OFF_H   = OFF_WOF + (size_t)D_ * D_ * 2;
constexpr size_t OFF_HNF = OFF_H + (size_t)S_ * D_ * 2;
constexpr size_t OFF_UF  = OFF_HNF + (size_t)S_ * D_ * 2;
constexpr size_t OFF_CNT = OFF_UF + (size_t)F_ * D_ * 2;

DI float bf2f(unsigned v) { return __uint_as_float(v << 16); }
DI float bflo(unsigned v) { return __uint_as_float(v << 16); }
DI float bfhi(unsigned v) { return __uint_as_float(v & 0xffff0000u); }
DI unsigned pack2(float a, float b) {
  typedef __attribute__((ext_vector_type(2))) __bf16 bf2;
  typedef __attribute__((ext_vector_type(2))) float f2;
  f2 v = {a, b};
  bf2 r = __builtin_convertvector(v, bf2);
  return __builtin_bit_cast(unsigned, r);
}
DI float bfr(float a) { return bflo(pack2(a, 0.f)); }
DI bf16x8 asbf(u32x4 v) { return __builtin_bit_cast(bf16x8, v); }

DI void glds16(const void* g, void* l) {
  __builtin_amdgcn_global_load_lds((const unsigned*)g, (unsigned*)l, 16, 0, 0);
}
DI void touch(const void* g, unsigned lds_dummy) {
  unsigned keep;
  asm volatile("s_mov_b32 %0, m0\n\ts_mov_b32 m0, %2\n\ts_nop 0\n\tglobal_load_lds_dword %1, off\n\ts_mov_b32 m0, %0"
               : "=&s"(keep) : "v"(g), "s"(lds_dummy) : "memory");
}
DI void store_sc1(void* g, u32x4 v) {
  asm volatile("global_store_dwordx4 %0, %1, off sc1\n\ts_nop 1" :: "v"(g), "v"(v) : "memory");
}
constexpr int NCNT_A = 16;
constexpr int CNT_STRIDE = 32;
DI void signal_counter(unsigned* c) {
  __hip_atomic_fetch_add(c, 1u, __ATOMIC_RELAXED, __HIP_MEMORY_SCOPE_AGENT);
}
DI void wait_counters(unsigned* c, int n, unsigned target, int l) {
  unsigned* p = c + (l < n ? l : 0) * CNT_STRIDE;
  for (int spin = 0; spin < 4000000; ++spin) {
    const unsigned v = __hip_atomic_load(p, __ATOMIC_RELAXED, __HIP_MEMORY_SCOPE_AGENT);
    if (__all(v >= target)) break;
    __builtin_amdgcn_s_sleep(1);
  }
}
DI s16x4 tr_read(const char* p) {
  return __builtin_amdgcn_ds_read_tr16_b64_v4i16((__attribute__((address_space(3))) s16x4*)(p));
}
#define STAMP(k, i) do {} while (0)
DI bf16x8 cat(s16x4 lo, s16x4 hi) { return __builtin_shufflevector(lo, hi, 0, 1, 2, 3, 4, 5, 6, 7); }

__global__ __launch_bounds__(256) void k1_ln_qkv(const u16* __restrict__ x, const u16* __restrict__ wq,
                                                 const u16* __restrict__ wk, const u16* __restrict__ wv,
                                                 const u16* __restrict__ wo, u16* __restrict__ Q,
                                                 u16* __restrict__ Kf, u16* __restrict__ Vf,
                                                 u16* __restrict__ WoF, unsigned* __restrict__ cnt) {
  __shared__ __attribute__((aligned(16))) char smem[D_ * 128 + 256];
  const int tid = threadIdx.x, l = tid & 63;
  const int w = __builtin_amdgcn_readfirstlane(tid >> 6);
  const int bx = blockIdx.x, xcd = bx & 7, ii = bx >> 3;
  const int tb = xcd + 8 * (ii >> 3), nt = ii & 7;
  const int g = l >> 4, i16 = l & 15;

  if (bx >= 256) {
    if (bx == 256 && tid < NCNT_A + 7) cnt[tid * CNT_STRIDE] = 0u;
#pragma unroll
    for (int r = 0; r < 2; ++r) {
      const int fi = (bx - 256) * 4 + w + 256 * r;
      if (fi < 392) {
        const int t = fi / 14, s = fi % 14;
        const u16* src = wo + (32 * s + 8 * g) * D_ + 16 * t + i16;
        u32x4 v;
#pragma unroll
        for (int j = 0; j < 4; ++j) v[j] = (unsigned)src[(2 * j) * D_] | ((unsigned)src[(2 * j + 1) * D_] << 16);
        const int dst = ((t / 7) * 2 + s / 7) * 49 + (s % 7) * 7 + t % 7;
        *(u32x4*)(WoF + ((size_t)dst * 64 + l) * 8) = v;
      }
    }
    return;
  }

  STAMP(0, 0);
  if (nt < 7) {
#pragma unroll
    for (int it = 0; it < 14; ++it) {
      const int piece = it * 4 + w;
      const int r = piece * 8 + (l >> 3);
      const int fr = ((r >> 1) & 1) | (((r >> 3) & 1) << 1);
      const int c = ((l & 7) >> 1) ^ fr;
      glds16(wq + r * D_ + nt * 64 + (2 * c + (l & 1)) * 8, smem + piece * 1024);
    }
  } else {
    const int col = (tid & 31) * 2, c16 = col & 31;
    const bool valid = c16 < DH_;
    const u16* wsrc = (col < 32 ? wk : wv) + (valid ? c16 : 0) + (tid >> 5) * DH_;
    unsigned v[56];
#pragma unroll
    for (int it = 0; it < 56; ++it) v[it] = *(const unsigned*)(wsrc + it * 8 * DH_);
#pragma unroll
    for (int it = 0; it < 56; ++it) {
      const int r = (tid >> 5) + 8 * it;
      const int fr = ((r >> 1) & 1) | (((r >> 3) & 1) << 1);
      *(unsigned*)(smem + r * 128 + (((col >> 4) ^ fr) * 32) + (col & 15) * 2) = valid ? v[it] : 0u;
    }
  }

  const int tok = tb * 64 + w * 16 + i16;
  u32x4 xf[14];
#pragma unroll
  for (int s = 0; s < 14; ++s) xf[s] = *(const u32x4*)(x + tok * D_ + 32 * s + 8 * g);
  const u32x4 ones = {0x3F803F80u, 0x3F803F80u, 0x3F803F80u, 0x3F803F80u};
  f32x4 accS = {0.f, 0.f, 0.f, 0.f}, accG = {0.f, 0.f, 0.f, 0.f};
#pragma unroll
  for (int s = 0; s < 14; ++s) {
    accS = MFMA16(asbf(ones), asbf(xf[s]), accS);
    accG = MFMA16(asbf(xf[s]), asbf(xf[s]), accG);
  }

  STAMP(0, 1);
  asm volatile("s_waitcnt vmcnt(0)" ::: "memory");
  __syncthreads();
  STAMP(0, 2);

  const int q4 = i16 >> 2, p4 = l & 3;
  const int fl = ((q4 >> 1) & 1) | ((g & 1) << 1);
  unsigned trb[4];
#pragma unroll
  for (int t = 0; t < 4; ++t) trb[t] = (unsigned)(size_t)smem + (8 * g + q4) * 128 + p4 * 8 + ((t ^ fl) * 32);
  f32x4 acc[4], accC = {0.f, 0.f, 0.f, 0.f};
#pragma unroll
  for (int t = 0; t < 4; ++t) acc[t] = (f32x4){0.f, 0.f, 0.f, 0.f};
  u32x2 wr[3][8];
#define K1_RD7(S) asm volatile( \
          "ds_read_b64_tr_b16 %0, %7 offset:%c11\n\t" \
          "ds_read_b64_tr_b16 %1, %7 offset:%c11+512\n\t" \
          "ds_read_b64_tr_b16 %2, %8 offset:%c11\n\t" \
          "ds_read_b64_tr_b16 %3, %8 offset:%c11+512\n\t" \
          "ds_read_b64_tr_b16 %4, %9 offset:%c11\n\t" \
          "ds_read_b64_tr_b16 %5, %9 offset:%c11+512\n\t" \
          "ds_read_b64_tr_b16 %6, %10 offset:%c11" \
          : "=&v"(wr[(S) % 3][0]), "=&v"(wr[(S) % 3][1]), "=&v"(wr[(S) % 3][2]), "=&v"(wr[(S) % 3][3]), \
            "=&v"(wr[(S) % 3][4]), "=&v"(wr[(S) % 3][5]), "=&v"(wr[(S) % 3][6]) \
          : "v"(trb[0]), "v"(trb[1]), "v"(trb[2]), "v"(trb[3]), "i"((S) * 4096) : "memory")
#define K1_RD8(S) asm volatile("ds_read_b64_tr_b16 %0, %1 offset:%c2" : "=&v"(wr[(S) % 3][7]) : "v"(trb[3]), "i"((S) * 4096 + 512) : "memory")
#define K1_WAIT(S, N) asm volatile("s_waitcnt lgkmcnt(%c8)" \
          : "+v"(wr[(S) % 3][0]), "+v"(wr[(S) % 3][1]), "+v"(wr[(S) % 3][2]), "+v"(wr[(S) % 3][3]), \
            "+v"(wr[(S) % 3][4]), "+v"(wr[(S) % 3][5]), "+v"(wr[(S) % 3][6]), "+v"(wr[(S) % 3][7]) : "i"(N) : "memory")
#define K1_WF(S, t) asbf((u32x4){wr[(S) % 3][2 * (t)][0], wr[(S) % 3][2 * (t)][1], wr[(S) % 3][2 * (t) + 1][0], wr[(S) % 3][2 * (t) + 1][1]})
#define K1_LOOP(KV) do { \
    _Pragma("unroll") for (int s = 0; s < 14; ++s) { \
      if (s < 12) { K1_RD7(s + 2); K1_WAIT(s, 15); K1_RD8(s + 2); } \
      else if (s == 12) { K1_WAIT(s, 8); } \
      else { K1_WAIT(s, 0); } \
      acc[0] = MFMA16(K1_WF(s, 0), asbf(xf[s]), acc[0]); \
      acc[1] = MFMA16(K1_WF(s, 1), asbf(xf[s]), acc[1]); \
      if (!(KV)) { \
        acc[2] = MFMA16(K1_WF(s, 2), asbf(xf[s]), acc[2]);        \
        acc[3] = MFMA16(K1_WF(s, 3), asbf(xf[s]), acc[3]); \
      } else { \
        acc[2] = MFMA16(asbf(xf[s]), K1_WF(s, 2), acc[2]);        \
        acc[3] = MFMA16(asbf(xf[s]), K1_WF(s, 3), acc[3]); \
      } \
    } } while (0)
  K1_RD7(0); K1_RD8(0); K1_RD7(1); K1_RD8(1);
  const bool kv = nt == 7;
  if (!kv) K1_LOOP(false); else K1_LOOP(true);
  {
    const unsigned cb = (unsigned)(size_t)smem + (8 * g + q4) * 128 + p4 * 8 + ((w ^ fl) * 32);
    u32x2 cr[14][2];
#pragma unroll
    for (int hs = 0; hs < 2; ++hs) {
#define CS_(i) "ds_read_b64_tr_b16 %" #i ", %14 offset:%c15+"
      asm volatile(
          "ds_read_b64_tr_b16 %0, %14 offset:%c15\n\t"       "ds_read_b64_tr_b16 %1, %14 offset:%c15+512\n\t"
          "ds_read_b64_tr_b16 %2, %14 offset:%c15+4096\n\t"  "ds_read_b64_tr_b16 %3, %14 offset:%c15+4608\n\t"
          "ds_read_b64_tr_b16 %4, %14 offset:%c15+8192\n\t"  "ds_read_b64_tr_b16 %5, %14 offset:%c15+8704\n\t"
          "ds_read_b64_tr_b16 %6, %14 offset:%c15+12288\n\t" "ds_read_b64_tr_b16 %7, %14 offset:%c15+12800\n\t"
          "ds_read_b64_tr_b16 %8, %14 offset:%c15+16384\n\t" "ds_read_b64_tr_b16 %9, %14 offset:%c15+16896\n\t"
          "ds_read_b64_tr_b16 %10, %14 offset:%c15+20480\n\t" "ds_read_b64_tr_b16 %11, %14 offset:%c15+20992\n\t"
          "ds_read_b64_tr_b16 %12, %14 offset:%c15+24576\n\t" "ds_read_b64_tr_b16 %13, %14 offset:%c15+25088\n\t"
          "s_waitcnt lgkmcnt(0)"
          : "=&v"(cr[7 * hs][0]), "=&v"(cr[7 * hs][1]), "=&v"(cr[7 * hs + 1][0]), "=&v"(cr[7 * hs + 1][1]),
            "=&v"(cr[7 * hs + 2][0]), "=&v"(cr[7 * hs + 2][1]), "=&v"(cr[7 * hs + 3][0]), "=&v"(cr[7 * hs + 3][1]),
            "=&v"(cr[7 * hs + 4][0]), "=&v"(cr[7 * hs + 4][1]), "=&v"(cr[7 * hs + 5][0]), "=&v"(cr[7 * hs + 5][1]),
            "=&v"(cr[7 * hs + 6][0]), "=&v"(cr[7 * hs + 6][1])
          : "v"(cb), "i"(hs * 7 * 4096) : "memory");
#undef CS_
    }
#pragma unroll
    for (int s = 0; s < 14; ++s)
      accC = MFMA16(asbf(ones), asbf((u32x4){cr[s][0][0], cr[s][0][1], cr[s][1][0], cr[s][1][1]}), accC);
  }
  __syncthreads();
  float* csb = (float*)smem;
  if (g == 0) csb[16 * w + i16] = accC[0];
  float mb, rs;
  {
    const int r3 = l & 3;
    const float cand = r3 == 0 ? accG[0] : r3 == 1 ? accG[1] : r3 == 2 ? accG[2] : accG[3];
    const float g2 = __shfl(cand, i16 + 16 * (i16 >> 2));
    const float mean = accS[0] * (1.0f / D_);
    const float var = fmaxf(g2 * (1.0f / D_) - mean * mean, 0.f);
    mb = bfr(mean);
    rs = __builtin_amdgcn_rcpf(bfr(__builtin_amdgcn_sqrtf(bfr(bfr(var) + 1e-5f))));
  }
  __syncthreads();
  if (!kv) {
    const float qs = rs * (0.18898223650461362f * 1.4426950408889634f);
#pragma unroll
    for (int t = 0; t < 4; ++t) {
      const int n = nt * 64 + 16 * t + 4 * g;
      const int head = n / DH_, d = n - head * DH_;
      const f32x4 cs = *(const f32x4*)(csb + 16 * t + 4 * g);
      u32x2 o;
      o[0] = pack2((acc[t][0] - mb * cs[0]) * qs, (acc[t][1] - mb * cs[1]) * qs);
      o[1] = pack2((acc[t][2] - mb * cs[2]) * qs, (acc[t][3] - mb * cs[3]) * qs);
      *(u32x2*)(Q + ((size_t)tok * H_ + head) * 32 + d) = o;
    }
  } else {
#pragma unroll
    for (int t = 0; t < 2; ++t) {
      const int d = 16 * t + 4 * g;
      const f32x4 cs = *(const f32x4*)(csb + d);
      u32x2 o;
      o[0] = pack2((acc[t][0] - mb * cs[0]) * rs, (acc[t][1] - mb * cs[1]) * rs);
      o[1] = pack2((acc[t][2] - mb * cs[2]) * rs, (acc[t][3] - mb * cs[3]) * rs);
      if (d == 28) { o[0] = 0x3F803F80u; o[1] = 0x00003F80u; }
      const int blk = tok >> 5, hh = (d & 15) >> 3, j = d & 7;
      *(u32x2*)(Kf + ((size_t)((blk * 2 + t) * 64 + (tok & 31) + 32 * hh)) * 8 + j) = o;
    }
    float mbk[4], rsk[4];
#pragma unroll
    for (int r = 0; r < 4; ++r) { mbk[r] = __shfl(mb, 4 * g + r); rsk[r] = __shfl(rs, 4 * g + r); }
#pragma unroll
    for (int t = 2; t < 4; ++t) {
      const int d = 16 * (t - 2) + i16;
      const float cs = csb[16 * t + i16];
      u32x2 o;
      o[0] = pack2((acc[t][0] - mbk[0] * cs) * rsk[0], (acc[t][1] - mbk[1] * cs) * rsk[1]);
      o[1] = pack2((acc[t][2] - mbk[2] * cs) * rsk[2], (acc[t][3] - mbk[3] * cs) * rsk[3]);
      if (d == 28) { o[0] = 0x3F803F80u; o[1] = 0x3F803F80u; }
      const int blk = tb * 2 + (w >> 1), s = w & 1;
      *(u32x2*)(Vf + ((size_t)((blk * 2 + s) * 64 + d + 32 * (g & 1))) * 8 + 4 * (g >> 1)) = o;
    }
  }
  STAMP(0, 3);
}

constexpr int PART_ROW = 144;
constexpr int PART_W = 128 * PART_ROW;
constexpr int K2_OBUF = 8 * PART_W;
constexpr int K2_DUMMY = K2_OBUF + 8 * D_ * 2;
constexpr int K2_SMEM = K2_DUMMY + 256;

DI float wave_sum(float v) {
  v += __builtin_bit_cast(float, __builtin_amdgcn_update_dpp(0, __builtin_bit_cast(int, v), 0x111, 0xf, 0xf, false));
  v += __builtin_bit_cast(float, __builtin_amdgcn_update_dpp(0, __builtin_bit_cast(int, v), 0x112, 0xf, 0xf, false));
  v += __builtin_bit_cast(float, __builtin_amdgcn_update_dpp(0, __builtin_bit_cast(int, v), 0x114, 0xf, 0xf, false));
  v += __builtin_bit_cast(float, __builtin_amdgcn_update_dpp(0, __builtin_bit_cast(int, v), 0x118, 0xf, 0xf, false));
  v += __builtin_bit_cast(float, __builtin_amdgcn_update_dpp(0, __builtin_bit_cast(int, v), 0x142, 0xa, 0xf, false));
  v += __builtin_bit_cast(float, __builtin_amdgcn_update_dpp(0, __builtin_bit_cast(int, v), 0x143, 0xc, 0xf, false));
  return __builtin_bit_cast(float, __builtin_amdgcn_readlane(__builtin_bit_cast(int, v), 63));
}

DI void attn_phase(char* smem, const u16* __restrict__ Q, const u16* __restrict__ Kf,
                   const u16* __restrict__ Vf, const u16* __restrict__ WoF, const u16* __restrict__ x,
                   u16* Hout, u16* HnF, const u16* __restrict__ w1, const u16* __restrict__ w2) {
  const int tid = threadIdx.x, l = tid & 63, h = l >> 5;
  const int w = __builtin_amdgcn_readfirstlane(tid >> 6);
  const int t0 = blockIdx.x * 8;

  STAMP(1, 0);
  if (w < 4) __builtin_amdgcn_s_setprio(2);
  const u32x4* kbase = (const u32x4*)Kf + (size_t)w * 8 * 128 + l;
  const u32x4* vbase = (const u32x4*)Vf + (size_t)w * 8 * 128 + l;
  u32x4 qf[4][2];
#pragma unroll
  for (int b = 0; b < 4; ++b)
#pragma unroll
    for (int s = 0; s < 2; ++s)
      qf[b][s] = *(const u32x4*)(Q + ((size_t)(t0 * H_ + 32 * b + (l & 31))) * 32 + 16 * s + 8 * h);

  const int role = (w + (blockIdx.x >> 3)) & 7;
  const int kh = role & 1, nq = role >> 1;
  const u32x4* wbase = (const u32x4*)WoF + (size_t)(role * 49) * 64 + l;
  constexpr int WO_EARLY = 0;
  u32x4 woe[WO_EARLY + 1];

  f32x16 zero16;
#pragma unroll
  for (int i = 0; i < 16; ++i) zero16[i] = 0.f;

  float mval[4];
  f32x16 o[4];
  int nmax = 1;
#pragma unroll
  for (int e = 0; e < WO_EARLY; ++e) woe[e] = wbase[e * 64];
  for (;;) {
    asm volatile("" : "+v"(kbase), "+v"(vbase));
    u32x4 k0n = kbase[0], k1n = kbase[64], v0n = vbase[0], v1n = vbase[64];
    u32x4 k0m = kbase[128], k1m = kbase[192], v0m = vbase[128], v1m = vbase[192];
    float mx[4];
#pragma unroll
    for (int b = 0; b < 4; ++b) {
      if (h) { qf[b][1][2] = 0u; qf[b][1][3] = 0u; }
      f32x16 s = MFMA32(asbf(k0n), asbf(qf[b][0]), zero16);
      s = MFMA32(asbf(k1n), asbf(qf[b][1]), s);
      float m = s[0];
#pragma unroll
      for (int i = 1; i < 16; ++i) m = fmaxf(m, s[i]);
      mx[b] = m;
    }
    for (int kb = 1; kb < nmax; ++kb) {
      const u32x4 k0 = kbase[kb * 128], k1 = kbase[kb * 128 + 64];
#pragma unroll
      for (int b = 0; b < 4; ++b) {
        f32x16 s = MFMA32(asbf(k0), asbf(qf[b][0]), zero16);
        s = MFMA32(asbf(k1), asbf(qf[b][1]), s);
#pragma unroll
        for (int i = 0; i < 16; ++i) mx[b] = fmaxf(mx[b], s[i]);
      }
    }
#pragma unroll
    for (int b = 0; b < 4; ++b) {
      const auto sw = __builtin_amdgcn_permlane32_swap(__float_as_uint(mx[b]), __float_as_uint(mx[b]), false, false);
      const float m = fmaxf(__uint_as_float(sw[0]), __uint_as_float(sw[1]));
      mval[b] = m;
      const float nm = -m;
      const unsigned hi = __float_as_uint(nm) & 0xffff0000u;
      const float r1 = nm - __uint_as_float(hi);
      const unsigned mid = __float_as_uint(r1) & 0xffff0000u;
      const float r2 = r1 - __uint_as_float(mid);
      const unsigned lo = __float_as_uint(r2) & 0xffff0000u;
      if (h) { qf[b][1][2] = (hi >> 16) | mid; qf[b][1][3] = lo >> 16; }
      o[b] = zero16;
    }
    STAMP(1, 1);
#define SB() __builtin_amdgcn_sched_barrier(0)
#define EXP4(S, i) do { S[i] = __builtin_amdgcn_exp2f(S[i]); S[i + 1] = __builtin_amdgcn_exp2f(S[i + 1]); \
                        S[i + 2] = __builtin_amdgcn_exp2f(S[i + 2]); S[i + 3] = __builtin_amdgcn_exp2f(S[i + 3]); } while (0)
#define STEP(SC, SN, B, BN, BP, KN0, KN1, V0, VP1) do { \
      u32x4 p0; \
      o[BP] = MFMA32(asbf(VP1), asbf(pp), o[BP]); SB(); \
      EXP4(SC, 0); SB(); \
      SN = MFMA32(asbf(KN0), asbf(qf[BN][0]), zero16); SB(); \
      EXP4(SC, 4); \
      p0[0] = pack2(SC[0], SC[1]); p0[1] = pack2(SC[2], SC[3]); p0[2] = pack2(SC[4], SC[5]); p0[3] = pack2(SC[6], SC[7]); SB(); \
      asm volatile("" :: "v"(pp)); \
      SN = MFMA32(asbf(KN1), asbf(qf[BN][1]), SN); SB(); \
      EXP4(SC, 8); SB(); \
      o[B] = MFMA32(asbf(V0), asbf(p0), o[B]); SB(); \
      EXP4(SC, 12); \
      pp[0] = pack2(SC[8], SC[9]); pp[1] = pack2(SC[10], SC[11]); pp[2] = pack2(SC[12], SC[13]); pp[3] = pack2(SC[14], SC[15]); SB(); \
      asm volatile("" :: "v"(p0)); \
    } while (0)
    {
      u32x4 kA0 = k0n, kA1 = k1n, vA0 = v0n, vA1 = v1n;
      u32x4 kB0 = k0m, kB1 = k1m, vB0 = v0m, vB1 = v1m;
      u32x4 kC0, kC1, vC0, vC1 = v1n;
      u32x4 pp = {0u, 0u, 0u, 0u};
      f32x16 sa, sb;
      sa = MFMA32(asbf(kA0), asbf(qf[0][0]), zero16);
      sa = MFMA32(asbf(kA1), asbf(qf[0][1]), sa);
#define BLOCK(KB, X, Y, Z, SA, SB_) do { \
        const int kn_ = ((KB) + 2) & 7; \
        k##Z##0 = kbase[kn_ * 128]; k##Z##1 = kbase[kn_ * 128 + 64]; v##Z##0 = vbase[kn_ * 128]; \
        SB(); \
        STEP(SA, SB_, 0, 1, 3, k##X##0, k##X##1, v##X##0, v##Z##1); \
        v##Z##1 = vbase[kn_ * 128 + 64]; \
        SB(); \
        STEP(SB_, SA, 1, 2, 0, k##X##0, k##X##1, v##X##0, v##X##1); \
        STEP(SA, SB_, 2, 3, 1, k##X##0, k##X##1, v##X##0, v##X##1); \
        STEP(SB_, SA, 3, 0, 2, k##Y##0, k##Y##1, v##X##0, v##X##1); \
      } while (0)
#pragma unroll 1
      for (int kb = 0; kb < 6; kb += 3) {
        BLOCK(kb, A, B, C, sa, sb);
        if (kb == 0 || w == 0) {
          const int job = kb == 0 ? w : 8;
          const int xcd = blockIdx.x & 7, bi = blockIdx.x >> 3;
          const u16* pf;
          if (job < 5) {
            const int idx = job * 64 + l, row = idx / 5, ln = idx - row * 5;
            int col = ((xcd * 49) >> 1) / 7 * 64 + ln * 64;
            col = col < F_ - 64 ? col : F_ - 64;
            pf = w1 + (size_t)(64 * bi + row) * F_ + col;
          } else {
            int idx = (job - 5) * 64 + l;
            idx = idx < 224 ? idx : 223;
            pf = w2 + (size_t)(56 * bi + (idx >> 2)) * S_ + 256 * xcd + (idx & 3) * 64;
          }
          touch(pf, (unsigned)(size_t)(smem + K2_DUMMY));
        }
        BLOCK(kb + 1, B, C, A, sa, sb);
        BLOCK(kb + 2, C, A, B, sa, sb);
      }
      BLOCK(6, A, B, C, sa, sb);
      BLOCK(7, B, C, A, sa, sb);
      o[3] = MFMA32(asbf(vB1), asbf(pp), o[3]);
    }
    bool ok = true;
#pragma unroll
    for (int b = 0; b < 4; ++b) ok = ok && (h == 0 || o[b][12] < 1.8446744e19f);
    if (nmax == 8 || __all(ok)) break;
    nmax = 8;
  }
  STAMP(1, 2);
  {
    char* pw = smem + w * PART_W;
#pragma unroll
    for (int b = 0; b < 4; ++b) {
      char* pr = pw + (32 * b + (l & 31)) * PART_ROW + 16 * h;
#pragma unroll
      for (int gq = 0; gq < 4; ++gq)
        *(f32x4*)(pr + 32 * gq) = (f32x4){o[b][4 * gq], o[b][4 * gq + 1], o[b][4 * gq + 2], o[b][4 * gq + 3]};
      if (h == 0) *(float*)(pr + 128) = mval[b];
    }
  }
  u32x4 xr;
  __builtin_amdgcn_s_setprio(0);
#define LOAD_WOF(S0, S1) do { \
    _Pragma("unroll") for (int s = S0; s < S1; ++s) \
      _Pragma("unroll") for (int i = 0; i < 7; ++i) \
        wof[s][i] = (s * 7 + i < WO_EARLY) ? woe[s * 7 + i] : wbase[(s * 7 + i) * 64]; \
  } while (0)
#define LOAD_XR() do { xr = *(const u32x4*)(x + (t0 + w) * D_ + 8 * (l < 56 ? l : 55)); } while (0)
#define WO_GEMM() do { \
    const int g = l >> 4; \
    f32x4 acc[7]; \
    _Pragma("unroll") for (int i = 0; i < 7; ++i) acc[i] = (f32x4){0.f, 0.f, 0.f, 0.f}; \
    _Pragma("unroll") for (int s = 0; s < 7; ++s) { \
      const u32x4 of = *(const u32x4*)(smem + K2_OBUF + (l & 7) * (D_ * 2) + 64 * (7 * kh + s) + 16 * g); \
      _Pragma("unroll") for (int i = 0; i < 7; ++i) acc[i] = MFMA16(asbf(wof[s][i]), asbf(of), acc[i]); \
    } \
    if ((l & 15) < 8) { \
      _Pragma("unroll") for (int i = 0; i < 7; ++i) \
        *(f32x4*)(smem + ((kh * 8 + (l & 15)) * D_ + 16 * (7 * nq + i) + 4 * g) * 4) = acc[i]; \
    } \
  } while (0)
  if (w < 4) {
    u32x4 wof[7][7];
    LOAD_WOF(0, 6);
    __builtin_amdgcn_sched_barrier(0);
    STAMP(1, 3);
    __syncthreads();
    STAMP(1, 4);
#pragma unroll 1
    for (int rep = 0; rep < 2; ++rep) {
      const int item = rep * 256 + tid;
      const int row = item >> 2, qd = item & 3;
      const char* pr0 = smem + row * PART_ROW;
      float M = -3.0e38f;
#pragma unroll
      for (int ww = 0; ww < 8; ++ww) M = fmaxf(M, *(const float*)(pr0 + ww * PART_W + 128));
      float lsum = 0.f;
      f32x4 a0 = {0.f, 0.f, 0.f, 0.f}, a1 = {0.f, 0.f, 0.f, 0.f};
#pragma unroll
      for (int ww = 0; ww < 8; ++ww) {
        const char* pr = pr0 + ww * PART_W;
        const float e = __builtin_amdgcn_exp2f(*(const float*)(pr + 128) - M);
        lsum += e * *(const float*)(pr + 112);
        a0 += e * *(const f32x4*)(pr + 32 * qd);
        a1 += e * *(const f32x4*)(pr + 32 * qd + 16);
      }
      const float inv = __builtin_amdgcn_rcpf(lsum);
      char* ob = smem + K2_OBUF + (row >> 4) * (D_ * 2) + (row & 15) * (DH_ * 2) + 16 * qd;
      u32x2 o0, o1;
      o0[0] = pack2(a0[0] * inv, a0[1] * inv); o0[1] = pack2(a0[2] * inv, a0[3] * inv);
      o1[0] = pack2(a1[0] * inv, a1[1] * inv); o1[1] = pack2(a1[2] * inv, a1[3] * inv);
      *(u32x2*)ob = o0;
      if (qd < 3) *(u32x2*)(ob + 8) = o1;
    }
    LOAD_WOF(6, 7);
    LOAD_XR();
    __builtin_amdgcn_sched_barrier(0);
    STAMP(1, 5);
    __syncthreads();
    STAMP(1, 6);
    WO_GEMM();
  } else {
    STAMP(1, 3);
    __syncthreads();
    STAMP(1, 4);
    u32x4 wof[7][7];
    LOAD_WOF(0, 7);
    LOAD_XR();
    __builtin_amdgcn_sched_barrier(0);
    STAMP(1, 5);
    __syncthreads();
    STAMP(1, 6);
    WO_GEMM();
  }
  STAMP(1, 7);
  __syncthreads();
  STAMP(1, 8);
  {
    const int lc = l < 56 ? l : 55;
    const float live = l < 56 ? 1.0f : 0.0f;
    const char* r0 = smem + (w * D_ + 8 * lc) * 4;
    const f32x4 a0 = *(const f32x4*)r0 + *(const f32x4*)(r0 + 8 * D_ * 4);
    const f32x4 a1 = *(const f32x4*)(r0 + 16) + *(const f32x4*)(r0 + 8 * D_ * 4 + 16);
    u32x4 hv;
    float v[8];
#pragma unroll
    for (int j = 0; j < 4; ++j) {
      const unsigned ab = j < 2 ? pack2(a0[2 * j], a0[2 * j + 1]) : pack2(a1[2 * j - 4], a1[2 * j - 3]);
      hv[j] = pack2(bflo(xr[j]) + bflo(ab), bfhi(xr[j]) + bfhi(ab));
      v[2 * j] = bflo(hv[j]); v[2 * j + 1] = bfhi(hv[j]);
    }
    if (l < 56) store_sc1(Hout + (t0 + w) * D_ + 8 * l, hv);
#pragma unroll
    for (int rep = 0; rep < 2; ++rep) {
      const float c = __builtin_bit_cast(float, __builtin_amdgcn_readfirstlane(__builtin_bit_cast(int, v[0])));
      float s1 = 0.f, s2 = 0.f;
#pragma unroll
      for (int i = 0; i < 8; ++i) { const float a = v[i] - c; s1 += a; s2 = fmaf(a, a, s2); }
      s1 = wave_sum(s1 * live) * (1.0f / D_);
      s2 = wave_sum(s2 * live) * (1.0f / D_);
      const float mean = c + s1;
      const float var = fmaxf(s2 - s1 * s1, 0.f);
      const float mb = bfr(mean);
      const float rs = __builtin_amdgcn_rcpf(bfr(__builtin_amdgcn_sqrtf(bfr(bfr(var) + 1e-5f))));
      const float nb = -mb * rs;
#pragma unroll
      for (int j = 0; j < 4; ++j) {
        hv[j] = pack2(fmaf(v[2 * j], rs, nb), fmaf(v[2 * j + 1], rs, nb));
        v[2 * j] = bflo(hv[j]); v[2 * j + 1] = bfhi(hv[j]);
      }
    }
    if (l < 56) *(u32x4*)(smem + K2_OBUF + (w * D_ + 8 * l) * 2) = hv;
  }
  STAMP(1, 9);
  __syncthreads();
  STAMP(1, 10);
  if (tid < D_) {
    const u16* hb = (const u16*)(smem + K2_OBUF);
    u32x4 v;
#pragma unroll
    for (int j = 0; j < 4; ++j)
      v[j] = (unsigned)hb[(2 * j) * D_ + tid] | ((unsigned)hb[(2 * j + 1) * D_ + tid] << 16);
    const int lane = ((t0 >> 3) & 3) * 16 + (tid & 15);
    store_sc1(HnF + ((size_t)((((tid >> 6) * 64 + (t0 >> 5)) * 4 + ((tid >> 4) & 3)) * 64 + lane)) * 8, v);
  }
  STAMP(1, 11);
}

constexpr int G_SMEM = 8 * 16384;
constexpr int GDEPTH = 4;

template <int MODE>
DI void gemm_phase(char* smem, int dt, int nt, const u16* __restrict__ W, const u16* act,
                   const u16* __restrict__ bias, const u16* Hres, u16* out,
                   unsigned* wait_cnt, int wait_n, unsigned wait_target) {
  constexpr int LDW = MODE == 0 ? F_ : S_;
  constexpr int NKS = MODE == 0 ? S_ / 32 : F_ / 32;
  constexpr int KPW = NKS / 8;
  const int tid = threadIdx.x, l = tid & 63;
  const int w = __builtin_amdgcn_readfirstlane(tid >> 6);
  char* ring = smem + w * (GDEPTH * 4096);
  const int g = l >> 4, i16 = l & 15, q4 = i16 >> 2, p4 = l & 3;
  const u16* asrc = act + ((size_t)dt * NKS + w * KPW) * 2048 + l * 8;
  const int fsrc = (l >> 4) & 1;
  const u16* wsrc = W + (size_t)(w * KPW * 32 + (l >> 3)) * LDW + 64 * nt + (l & 1) * 8;
  const int c0 = (l & 7) >> 1;
  const int ecol = tid >> 3, er8 = (tid & 7) * 8;
  const unsigned ring_a = (unsigned)(size_t)ring;

  u32x4 ar[GDEPTH][4];
  auto issueW = [&](int i) {
#pragma unroll
    for (int j = 0; j < 4; ++j) {
      const int c = c0 ^ (fsrc | ((j & 1) << 1));
      unsigned keep;
      asm volatile("s_mov_b32 %0, m0\n\ts_mov_b32 m0, %2\n\ts_nop 0\n\tglobal_load_lds_dwordx4 %1, off\n\ts_mov_b32 m0, %0"
                   : "=&s"(keep) : "v"(wsrc + (size_t)(i * 32 + 8 * j) * LDW + c * 16),
                     "s"(ring_a + (i % GDEPTH) * 4096 + j * 1024) : "memory");
    }
  };
  auto issueA = [&](int i) {
#pragma unroll
    for (int j = 0; j < 4; ++j)
      asm volatile("global_load_dwordx4 %0, %1, off sc1" : "=v"(ar[i % GDEPTH][j]) : "v"(asrc + (size_t)(i * 4 + j) * 512) : "memory");
  };

  const int fl = ((q4 >> 1) & 1) | ((g & 1) << 1);
  unsigned trb[4];
#pragma unroll
  for (int t = 0; t < 4; ++t) trb[t] = ring_a + (8 * g + q4) * 128 + p4 * 8 + ((t ^ fl) * 32);
#define G_TRREAD(DST, SL, N, ...) asm volatile( \
          "s_waitcnt vmcnt(%c10)\n\t" \
          "ds_read_b64_tr_b16 %0, %11 offset:%c8\n\t" \
          "ds_read_b64_tr_b16 %1, %11 offset:%c9\n\t" \
          "ds_read_b64_tr_b16 %2, %12 offset:%c8\n\t" \
          "ds_read_b64_tr_b16 %3, %12 offset:%c9\n\t" \
          "ds_read_b64_tr_b16 %4, %13 offset:%c8\n\t" \
          "ds_read_b64_tr_b16 %5, %13 offset:%c9\n\t" \
          "ds_read_b64_tr_b16 %6, %14 offset:%c8\n\t" \
          "ds_read_b64_tr_b16 %7, %14 offset:%c9\n\t" \
          "s_waitcnt lgkmcnt(0)" \
          : "=&v"(DST[0]), "=&v"(DST[1]), "=&v"(DST[2]), "=&v"(DST[3]), "=&v"(DST[4]), "=&v"(DST[5]), "=&v"(DST[6]), "=&v"(DST[7]) \
          : "i"((SL) * 4096), "i"((SL) * 4096 + 512), "i"(N), "v"(trb[0]), "v"(trb[1]), "v"(trb[2]), "v"(trb[3]) : "memory")
#define G_WF(R, t) asbf((u32x4){R[2 * (t)][0], R[2 * (t)][1], R[2 * (t) + 1][0], R[2 * (t) + 1][1]})

  STAMP(2 + MODE, 0);
  u32x2 wreg[GDEPTH][8];
#pragma unroll
  for (int i = 0; i < GDEPTH; ++i) issueW(i);
#pragma unroll
  for (int i = 0; i < GDEPTH; ++i) {
    G_TRREAD(wreg[i], i, 4 * (GDEPTH - 1 - i) + 4 * ((i + GDEPTH <= KPW ? i : KPW - GDEPTH)));
    if (i + GDEPTH < KPW) issueW(i + GDEPTH);
  }
  if (w == 0) wait_counters(wait_cnt, wait_n, wait_target, l);
  asm volatile("s_waitcnt lgkmcnt(0)" ::: "memory");
  __builtin_amdgcn_s_barrier();
  asm volatile("" ::: "memory");
  STAMP(2 + MODE, 1);
#pragma unroll
  for (int i = 0; i < GDEPTH; ++i) issueA(i);

  f32x4 acc[4][4];
#pragma unroll
  for (int a = 0; a < 4; ++a)
#pragma unroll
    for (int b = 0; b < 4; ++b) acc[a][b] = (f32x4){0.f, 0.f, 0.f, 0.f};

  u32x4 ep;
  unsigned epb = 0;
#pragma unroll
  for (int i = 0; i < KPW; ++i) {
    const int full = (KPW - 1 - i) < (GDEPTH - 1) ? (KPW - 1 - i) : (GDEPTH - 1);
    const int extra = (i == KPW - 1) ? (MODE == 0 ? 1 : 2) : 0;
    const int pend = full * 4 + extra;
    if (i == KPW - 1) {
      if (MODE == 0) {
        asm volatile("global_load_dwordx4 %0, %1, off" : "=v"(ep) : "v"(bias + 64 * nt + er8) : "memory");
      } else {
        asm volatile("global_load_dwordx4 %0, %1, off sc1" : "=v"(ep) : "v"(Hres + (size_t)(64 * nt + ecol) * D_ + 64 * dt + er8) : "memory");
        asm volatile("global_load_ushort %0, %1, off" : "=v"(epb) : "v"(bias + 64 * nt + ecol) : "memory");
      }
    }
    u32x4 a0 = ar[i % GDEPTH][0], a1 = ar[i % GDEPTH][1], a2 = ar[i % GDEPTH][2], a3 = ar[i % GDEPTH][3];
    u32x2 wl[8];
    if (i >= GDEPTH) G_TRREAD(wl, i - GDEPTH, 63);
    asm volatile("s_waitcnt vmcnt(%c4)" : "+v"(a0), "+v"(a1), "+v"(a2), "+v"(a3) : "i"(pend) : "memory");
    if (i + GDEPTH < KPW) issueA(i + GDEPTH);
    const bf16x8 af[4] = {asbf(a0), asbf(a1), asbf(a2), asbf(a3)};
    bf16x8 wf[4];
#pragma unroll
    for (int t = 0; t < 4; ++t) wf[t] = i < GDEPTH ? G_WF(wreg[i % GDEPTH], t) : G_WF(wl, t);
#pragma unroll
    for (int a = 0; a < 4; ++a)
#pragma unroll
      for (int b = 0; b < 4; ++b) {
        if (MODE == 0) acc[a][b] = MFMA16(wf[a], af[b], acc[a][b]);
        else           acc[a][b] = MFMA16(af[a], wf[b], acc[a][b]);
      }
  }
  STAMP(2 + MODE, 2);
  {
#pragma unroll
    for (int a = 0; a < 4; ++a)
#pragma unroll
      for (int b = 0; b < 4; ++b)
        *(f32x4*)(ring + (16 * b + i16) * 256 + (((4 * a + g) ^ i16) * 16)) = acc[a][b];
  }
  asm volatile("s_waitcnt vmcnt(0)" : "+v"(ep), "+v"(epb) :: "memory");
  STAMP(2 + MODE, 3);
  __syncthreads();
  {
    f32x4 s0 = {0.f, 0.f, 0.f, 0.f}, s1 = {0.f, 0.f, 0.f, 0.f};
    const int ch = (tid & 7) * 2;
    const char* p0 = smem + ecol * 256 + ((ch ^ (ecol & 15)) * 16);
    const char* p1 = smem + ecol * 256 + (((ch + 1) ^ (ecol & 15)) * 16);
#pragma unroll
    for (int ww = 0; ww < 8; ++ww) {
      s0 += *(const f32x4*)(p0 + ww * 16384);
      s1 += *(const f32x4*)(p1 + ww * 16384);
    }
    float v[8] = {s0[0], s0[1], s0[2], s0[3], s1[0], s1[1], s1[2], s1[3]};
    u32x4 ov;
    if (MODE == 0) {
      const int d = 64 * dt + ecol, f = 64 * nt + er8;
#pragma unroll
      for (int j = 0; j < 8; ++j) {
        const float bb = (j & 1) ? bfhi(ep[j >> 1]) : bflo(ep[j >> 1]);
        const float t = bfr(bfr(v[j]) + bb);
        const float y = 0.7978845608028654f * (t + 0.044715f * t * t * t);
        v[j] = t * __builtin_amdgcn_rcpf(1.0f + __expf(-2.0f * y));
      }
#pragma unroll
      for (int j = 0; j < 4; ++j) ov[j] = pack2(v[2 * j], v[2 * j + 1]);
      const int lane = ((f & 31) >> 3) * 16 + (d & 15);
      store_sc1(out + ((size_t)(((d >> 6) * (F_ / 32) + (f >> 5)) * 4 + ((d >> 4) & 3)) * 64 + lane) * 8, ov);
    } else {
      const int s = 64 * nt + ecol, d = 64 * dt + er8;
      const float bb = bf2f(epb);
#pragma unroll
      for (int j = 0; j < 8; ++j) {
        const float hh = (j & 1) ? bfhi(ep[j >> 1]) : bflo(ep[j >> 1]);
        v[j] = hh + bfr(bfr(v[j]) + bb);
      }
#pragma unroll
      for (int j = 0; j < 4; ++j) ov[j] = pack2(v[2 * j], v[2 * j + 1]);
      *(u32x4*)(out + (size_t)s * D_ + d) = ov;
    }
  }
  STAMP(2 + MODE, 4);
}

__global__ __launch_bounds__(512, 2) void k_fused(const u16* __restrict__ Q, const u16* __restrict__ Kf,
                                                  const u16* __restrict__ Vf, const u16* __restrict__ WoF,
                                                  const u16* __restrict__ x, u16* Hb, u16* HnF, u16* UF,
                                                  const u16* __restrict__ w1, const u16* __restrict__ b1,
                                                  const u16* __restrict__ w2, const u16* __restrict__ b2,
                                                  u16* out, unsigned* cnt) {
  extern __shared__ __attribute__((aligned(16))) char smem[];
  const int tid = threadIdx.x;
  const int xcd = blockIdx.x & 7, bi = blockIdx.x >> 3;
  unsigned* cntA = cnt;
  unsigned* cntB = cnt + NCNT_A * CNT_STRIDE;

  attn_phase(smem, Q, Kf, Vf, WoF, x, Hb, HnF, w1, w2);
  asm volatile("s_waitcnt vmcnt(0)" ::: "memory");
  __syncthreads();
  if (tid == 0) signal_counter(cntA + (blockIdx.x & (NCNT_A - 1)) * CNT_STRIDE);

  {
    const int start = (xcd * 49) >> 1, cntx = ((xcd + 1) * 49 >> 1) - start;
    if (bi < cntx) {
      const int j = start + bi, nt = j / 7, dt = j - nt * 7;
      gemm_phase<0>(smem, dt, nt, w1, HnF, b1, nullptr, UF, cntA, NCNT_A, 256 / NCNT_A);
      asm volatile("s_waitcnt vmcnt(0)" ::: "memory");
      __syncthreads();
      if (tid == 0) signal_counter(cntB + dt * CNT_STRIDE);
    }
  }
  if (bi < 28) {
    const int j = xcd * 28 + bi, nt = j / 7, dt = j - nt * 7;
    gemm_phase<1>(smem, dt, nt, w2, UF, b2, Hb, out, cntB + dt * CNT_STRIDE, 1, 28);
  }
}

extern "C" void kernel_launch(void* const* d_in, const int* in_sizes, int n_in, void* d_out, int out_size,
                              void* d_ws, size_t ws_size, hipStream_t stream) {
  const u16* x  = (const u16*)d_in[0];
  const u16* wq = (const u16*)d_in[1];
  const u16* wk = (const u16*)d_in[2];
  const u16* wv = (const u16*)d_in[3];
  const u16* wo = (const u16*)d_in[4];
  const u16* w1 = (const u16*)d_in[6];
  const u16* b1 = (const u16*)d_in[7];
  const u16* w2 = (const u16*)d_in[8];
  const u16* b2 = (const u16*)d_in[9];
  char* ws = (char*)d_ws;
  u16* Q   = (u16*)(ws + OFF_Q);
  u16* Kf  = (u16*)(ws + OFF_KF);
  u16* Vf  = (u16*)(ws + OFF_VF);
  u16* WoF = (u16*)(ws + OFF_WOF);
  u16* Hb  = (u16*)(ws + OFF_H);
  u16* HnF = (u16*)(ws + OFF_HNF);
  u16* UF  = (u16*)(ws + OFF_UF);

  unsigned* cnt = (unsigned*)(ws + OFF_CNT);

  (void)hipFuncSetAttribute((const void*)k_fused, hipFuncAttributeMaxDynamicSharedMemorySize, K2_SMEM);

  k1_ln_qkv<<<320, 256, 0, stream>>>(x, wq, wk, wv, wo, Q, Kf, Vf, WoF, cnt);
  k_fused<<<256, 512, K2_SMEM, stream>>>(Q, Kf, Vf, WoF, x, Hb, HnF, UF, w1, b1, w2, b2, (u16*)d_out, cnt);
}
```

```cpp
#include <hip/hip_runtime.h>
#include <hip/hip_bf16.h>
#include <stdint.h>

typedef unsigned short u16;
typedef __attribute__((ext_vector_type(8))) short bf16x8;
typedef __attribute__((ext_vector_type(4))) short s16x4;
typedef __attribute__((ext_vector_type(4))) float f32x4;
typedef __attribute__((ext_vector_type(16))) float f32x16;
typedef __attribute__((ext_vector_type(4))) unsigned u32x4;
typedef __attribute__((ext_vector_type(2))) unsigned u32x2;

#define DI __device__ __forceinline__
#define MFMA32(a, b, c) __builtin_amdgcn_mfma_f32_32x32x16_bf16((a), (b), (c), 0, 0, 0)
#define MFMA16(a, b, c) __builtin_amdgcn_mfma_f32_16x16x32_bf16((a), (b), (c), 0, 0, 0)

constexpr int S_ = 2048, D_ = 448, H_ = 16, DH_ = 28, F_ = 1792;

constexpr size_t OFF_Q   = 0;
constexpr size_t OFF_KF  = OFF_Q + (size_t)S_ * H_ * 32 * 2;
constexpr size_t OFF_VF  = OFF_KF + (size_t)S_ * 32 * 2;
constexpr size_t OFF_WOF = OFF_VF + (size_t)S_ * 32 * 2;
constexpr size_t OFF_H   = OFF_WOF + (size_t)D_ * D_ * 2;
constexpr size_t OFF_HNF = OFF_H + (size_t)S_ * D_ * 2;
constexpr size_t OFF_UF  = OFF_HNF + (size_t)S_ * D_ * 2;
constexpr size_t OFF_CNT = OFF_UF + (size_t)F_ * D_ * 2;

DI float bf2f(unsigned v) { return __uint_as_float(v << 16); }
DI float bflo(unsigned v) { return __uint_as_float(v << 16); }
DI float bfhi(unsigned v) { return __uint_as_float(v & 0xffff0000u); }
DI unsigned pack2(float a, float b) {
  typedef __attribute__((ext_vector_type(2))) __bf16 bf2;
  typedef __attribute__((ext_vector_type(2))) float f2;
  f2 v = {a, b};
  bf2 r = __builtin_convertvector(v, bf2);
  return __builtin_bit_cast(unsigned, r);
}
DI float bfr(float a) { return bflo(pack2(a, 0.f)); }
DI bf16x8 asbf(u32x4 v) { return __builtin_bit_cast(bf16x8, v); }

DI void glds16(const void* g, void* l) {
  __builtin_amdgcn_global_load_lds((const unsigned*)g, (unsigned*)l, 16, 0, 0);
}
DI void touch(const void* g, unsigned lds_dummy) {
  unsigned keep;
  asm volatile("s_mov_b32 %0, m0\n\ts_mov_b32 m0, %2\n\ts_nop 0\n\tglobal_load_lds_dword %1, off\n\ts_mov_b32 m0, %0"
               : "=&s"(keep) : "v"(g), "s"(lds_dummy) : "memory");
}
DI void store_sc1(void* g, u32x4 v) {
  asm volatile("global_store_dwordx4 %0, %1, off sc1\n\ts_nop 1" :: "v"(g), "v"(v) : "memory");
}
constexpr int NCNT_A = 16;
constexpr int CNT_STRIDE = 32;
DI void signal_counter(unsigned* c) {
  __hip_atomic_fetch_add(c, 1u, __ATOMIC_RELAXED, __HIP_MEMORY_SCOPE_AGENT);
}
DI void wait_counters(unsigned* c, int n, unsigned target, int l) {
  unsigned* p = c + (l < n ? l : 0) * CNT_STRIDE;
  for (int spin = 0; spin < 4000000; ++spin) {
    const unsigned v = __hip_atomic_load(p, __ATOMIC_RELAXED, __HIP_MEMORY_SCOPE_AGENT);
    if (__all(v >= target)) break;
    __builtin_amdgcn_s_sleep(1);
  }
}
DI s16x4 tr_read(const char* p) {
  return __builtin_amdgcn_ds_read_tr16_b64_v4i16((__attribute__((address_space(3))) s16x4*)(p));
}
#define STAMP(k, i) do {} while (0)
DI bf16x8 cat(s16x4 lo, s16x4 hi) { return __builtin_shufflevector(lo, hi, 0, 1, 2, 3, 4, 5, 6, 7); }

__global__ __launch_bounds__(256) void k1_ln_qkv(const u16* __restrict__ x, const u16* __restrict__ wq,
                                                 const u16* __restrict__ wk, const u16* __restrict__ wv,
                                                 const u16* __restrict__ wo, u16* __restrict__ Q,
                                                 u16* __restrict__ Kf, u16* __restrict__ Vf,
                                                 u16* __restrict__ WoF, unsigned* __restrict__ cnt) {
  __shared__ __attribute__((aligned(16))) char smem[2 * D_ * 128];
  const int tid = threadIdx.x, l = tid & 63;
  const int w = __builtin_amdgcn_readfirstlane(tid >> 6);
  const int bx = blockIdx.x, xcd = bx & 7, ii = bx >> 3;
  const int tb = xcd + 8 * (ii >> 3), nt = ii & 7;
  const int g = l >> 4, i16 = l & 15;

  STAMP(0, 0);
  if (bx == 0 && tid < NCNT_A + 7) cnt[tid * CNT_STRIDE] = 0u;
  unsigned wov[8];
  const bool do_wo = w < 2 && bx < 196;
  const int fi = do_wo ? 2 * bx + w : 0;
  {
    const u16* src = wo + (32 * (fi % 14) + 8 * g) * D_ + 16 * (fi / 14) + i16;
#pragma unroll
    for (int j = 0; j < 8; ++j)
      asm volatile("global_load_ushort %0, %1, off offset:%c2" : "=v"(wov[j]) : "v"(src + (j >> 2) * 4 * D_), "i"((j & 3) * D_ * 2) : "memory");
  }
  {
    const u16* xs = x + (size_t)(tb * 64 + w * 16) * D_;
    int row = l >= 56 ? 1 : 0, pos = l >= 56 ? l - 56 : l;
#pragma unroll
    for (int it = 0; it < 14; ++it) {
      glds16(xs + row * D_ + ((pos & ~7) | ((pos ^ row) & 7)) * 8, smem + D_ * 128 + w * 14336 + it * 1024);
      pos += 8; row += 1;
      if (pos >= 56) { pos -= 56; row += 1; }
    }
  }
  if (nt < 7) {
#pragma unroll
    for (int it = 0; it < 14; ++it) {
      const int piece = it * 4 + w;
      const int r = piece * 8 + (l >> 3);
      const int fr = ((r >> 1) & 1) | (((r >> 3) & 1) << 1);
      const int c = ((l & 7) >> 1) ^ fr;
      glds16(wq + r * D_ + nt * 64 + (2 * c + (l & 1)) * 8, smem + piece * 1024);
    }
  } else {
    const int col = (tid & 31) * 2, c16 = col & 31;
    const bool valid = c16 < DH_;
    const u16* wsrc = (col < 32 ? wk : wv) + (valid ? c16 : 0) + (tid >> 5) * DH_;
    unsigned v[56];
#pragma unroll
    for (int it = 0; it < 56; ++it) v[it] = *(const unsigned*)(wsrc + it * 8 * DH_);
#pragma unroll
    for (int it = 0; it < 56; ++it) {
      const int r = (tid >> 5) + 8 * it;
      const int fr = ((r >> 1) & 1) | (((r >> 3) & 1) << 1);
      *(unsigned*)(smem + r * 128 + (((col >> 4) ^ fr) * 32) + (col & 15) * 2) = valid ? v[it] : 0u;
    }
  }

  const int tok = tb * 64 + w * 16 + i16;
  STAMP(0, 1);
  asm volatile("s_waitcnt vmcnt(0)" : "+v"(wov[0]), "+v"(wov[1]), "+v"(wov[2]), "+v"(wov[3]), "+v"(wov[4]), "+v"(wov[5]),
               "+v"(wov[6]), "+v"(wov[7]) :: "memory");
  __syncthreads();
  STAMP(0, 2);
  u32x4 xf[14];
#pragma unroll
  for (int s = 0; s < 14; ++s) {
    const int c = 4 * s + g;
    xf[s] = *(const u32x4*)(smem + D_ * 128 + w * 14336 + i16 * 896 + ((c & ~7) | ((c ^ i16) & 7)) * 16);
  }
  const u32x4 ones = {0x3F803F80u, 0x3F803F80u, 0x3F803F80u, 0x3F803F80u};
  f32x4 accS = {0.f, 0.f, 0.f, 0.f}, accG = {0.f, 0.f, 0.f, 0.f};
  asm volatile("s_waitcnt lgkmcnt(0)" : "+v"(xf[0]), "+v"(xf[1]), "+v"(xf[2]), "+v"(xf[3]), "+v"(xf[4]), "+v"(xf[5]), "+v"(xf[6]),
               "+v"(xf[7]), "+v"(xf[8]), "+v"(xf[9]), "+v"(xf[10]), "+v"(xf[11]), "+v"(xf[12]), "+v"(xf[13]) :: "memory");

  const int q4 = i16 >> 2, p4 = l & 3;
  const int fl = ((q4 >> 1) & 1) | ((g & 1) << 1);
  unsigned trb[4];
#pragma unroll
  for (int t = 0; t < 4; ++t) trb[t] = (unsigned)(size_t)smem + (8 * g + q4) * 128 + p4 * 8 + ((t ^ fl) * 32);
  f32x4 acc[4], accC = {0.f, 0.f, 0.f, 0.f};
#pragma unroll
  for (int t = 0; t < 4; ++t) acc[t] = (f32x4){0.f, 0.f, 0.f, 0.f};
  u32x2 wr[3][8];
#define K1_RD7(S) asm volatile( \
          "ds_read_b64_tr_b16 %0, %7 offset:%c11\n\t" \
          "ds_read_b64_tr_b16 %1, %7 offset:%c11+512\n\t" \
          "ds_read_b64_tr_b16 %2, %8 offset:%c11\n\t" \
          "ds_read_b64_tr_b16 %3, %8 offset:%c11+512\n\t" \
          "ds_read_b64_tr_b16 %4, %9 offset:%c11\n\t" \
          "ds_read_b64_tr_b16 %5, %9 offset:%c11+512\n\t" \
          "ds_read_b64_tr_b16 %6, %10 offset:%c11" \
          : "=&v"(wr[(S) % 3][0]), "=&v"(wr[(S) % 3][1]), "=&v"(wr[(S) % 3][2]), "=&v"(wr[(S) % 3][3]), \
            "=&v"(wr[(S) % 3][4]), "=&v"(wr[(S) % 3][5]), "=&v"(wr[(S) % 3][6]) \
          : "v"(trb[0]), "v"(trb[1]), "v"(trb[2]), "v"(trb[3]), "i"((S) * 4096) : "memory")
#define K1_RD8(S) asm volatile("ds_read_b64_tr_b16 %0, %1 offset:%c2" : "=&v"(wr[(S) % 3][7]) : "v"(trb[3]), "i"((S) * 4096 + 512) : "memory")
#define K1_WAIT(S, N) asm volatile("s_waitcnt lgkmcnt(%c8)" \
          : "+v"(wr[(S) % 3][0]), "+v"(wr[(S) % 3][1]), "+v"(wr[(S) % 3][2]), "+v"(wr[(S) % 3][3]), \
            "+v"(wr[(S) % 3][4]), "+v"(wr[(S) % 3][5]), "+v"(wr[(S) % 3][6]), "+v"(wr[(S) % 3][7]) : "i"(N) : "memory")
#define K1_WF(S, t) asbf((u32x4){wr[(S) % 3][2 * (t)][0], wr[(S) % 3][2 * (t)][1], wr[(S) % 3][2 * (t) + 1][0], wr[(S) % 3][2 * (t) + 1][1]})
#define K1_LOOP(KV) do { \
    _Pragma("unroll") for (int s = 0; s < 14; ++s) { \
      if (s < 12) { K1_RD7(s + 2); K1_WAIT(s, 15); K1_RD8(s + 2); } \
      else if (s == 12) { K1_WAIT(s, 8); } \
      else { K1_WAIT(s, 0); } \
      accS = MFMA16(asbf(ones), asbf(xf[s]), accS);        \
      accG = MFMA16(asbf(xf[s]), asbf(xf[s]), accG);       \
      acc[0] = MFMA16(K1_WF(s, 0), asbf(xf[s]), acc[0]); \
      acc[1] = MFMA16(K1_WF(s, 1), asbf(xf[s]), acc[1]); \
      if (!(KV)) { \
        acc[2] = MFMA16(K1_WF(s, 2), asbf(xf[s]), acc[2]);        \
        acc[3] = MFMA16(K1_WF(s, 3), asbf(xf[s]), acc[3]); \
      } else { \
        acc[2] = MFMA16(asbf(xf[s]), K1_WF(s, 2), acc[2]);        \
        acc[3] = MFMA16(asbf(xf[s]), K1_WF(s, 3), acc[3]); \
      } \
    } } while (0)
  K1_RD7(0); K1_RD8(0); K1_RD7(1); K1_RD8(1);
  const bool kv = nt == 7;
  if (!kv) K1_LOOP(false); else K1_LOOP(true);
  {
    const unsigned cb = (unsigned)(size_t)smem + (8 * g + q4) * 128 + p4 * 8 + ((w ^ fl) * 32);
    u32x2 cr[14][2];
#pragma unroll
    for (int hs = 0; hs < 2; ++hs) {
#define CS_(i) "ds_read_b64_tr_b16 %" #i ", %14 offset:%c15+"
      asm volatile(
          "ds_read_b64_tr_b16 %0, %14 offset:%c15\n\t"       "ds_read_b64_tr_b16 %1, %14 offset:%c15+512\n\t"
          "ds_read_b64_tr_b16 %2, %14 offset:%c15+4096\n\t"  "ds_read_b64_tr_b16 %3, %14 offset:%c15+4608\n\t"
          "ds_read_b64_tr_b16 %4, %14 offset:%c15+8192\n\t"  "ds_read_b64_tr_b16 %5, %14 offset:%c15+8704\n\t"
          "ds_read_b64_tr_b16 %6, %14 offset:%c15+12288\n\t" "ds_read_b64_tr_b16 %7, %14 offset:%c15+12800\n\t"
          "ds_read_b64_tr_b16 %8, %14 offset:%c15+16384\n\t" "ds_read_b64_tr_b16 %9, %14 offset:%c15+16896\n\t"
          "ds_read_b64_tr_b16 %10, %14 offset:%c15+20480\n\t" "ds_read_b64_tr_b16 %11, %14 offset:%c15+20992\n\t"
          "ds_read_b64_tr_b16 %12, %14 offset:%c15+24576\n\t" "ds_read_b64_tr_b16 %13, %14 offset:%c15+25088\n\t"
          "s_waitcnt lgkmcnt(0)"
          : "=&v"(cr[7 * hs][0]), "=&v"(cr[7 * hs][1]), "=&v"(cr[7 * hs + 1][0]), "=&v"(cr[7 * hs + 1][1]),
            "=&v"(cr[7 * hs + 2][0]), "=&v"(cr[7 * hs + 2][1]), "=&v"(cr[7 * hs + 3][0]), "=&v"(cr[7 * hs + 3][1]),
            "=&v"(cr[7 * hs + 4][0]), "=&v"(cr[7 * hs + 4][1]), "=&v"(cr[7 * hs + 5][0]), "=&v"(cr[7 * hs + 5][1]),
            "=&v"(cr[7 * hs + 6][0]), "=&v"(cr[7 * hs + 6][1])
          : "v"(cb), "i"(hs * 7 * 4096) : "memory");
#undef CS_
    }
#pragma unroll
    for (int s = 0; s < 14; ++s)
      accC = MFMA16(asbf(ones), asbf((u32x4){cr[s][0][0], cr[s][0][1], cr[s][1][0], cr[s][1][1]}), accC);
  }
  __syncthreads();
  float* csb = (float*)smem;
  if (g == 0) csb[16 * w + i16] = accC[0];
  float mb, rs;
  {
    const int r3 = l & 3;
    const float cand = r3 == 0 ? accG[0] : r3 == 1 ? accG[1] : r3 == 2 ? accG[2] : accG[3];
    const float g2 = __shfl(cand, i16 + 16 * (i16 >> 2));
    const float mean = accS[0] * (1.0f / D_);
    const float var = fmaxf(g2 * (1.0f / D_) - mean * mean, 0.f);
    mb = bfr(mean);
    rs = __builtin_amdgcn_rcpf(bfr(__builtin_amdgcn_sqrtf(bfr(bfr(var) + 1e-5f))));
  }
  __syncthreads();
  if (!kv) {
    const float qs = rs * (0.18898223650461362f * 1.4426950408889634f);
#pragma unroll
    for (int t = 0; t < 4; ++t) {
      const int n = nt * 64 + 16 * t + 4 * g;
      const int head = n / DH_, d = n - head * DH_;
      const f32x4 cs = *(const f32x4*)(csb + 16 * t + 4 * g);
      u32x2 o;
      o[0] = pack2((acc[t][0] - mb * cs[0]) * qs, (acc[t][1] - mb * cs[1]) * qs);
      o[1] = pack2((acc[t][2] - mb * cs[2]) * qs, (acc[t][3] - mb * cs[3]) * qs);
      const int r = (tok & 7) * 16 + head;
      *(u32x2*)(Q + ((size_t)((((tok >> 3) * 4 + (r >> 5)) * 2 + (d >> 4)) * 64 + (r & 31) + 32 * ((d >> 3) & 1))) * 8 + (d & 7)) = o;
    }
  } else {
#pragma unroll
    for (int t = 0; t < 2; ++t) {
      const int d = 16 * t + 4 * g;
      const f32x4 cs = *(const f32x4*)(csb + d);
      u32x2 o;
      o[0] = pack2((acc[t][0] - mb * cs[0]) * rs, (acc[t][1] - mb * cs[1]) * rs);
      o[1] = pack2((acc[t][2] - mb * cs[2]) * rs, (acc[t][3] - mb * cs[3]) * rs);
      if (d == 28) { o[0] = 0x3F803F80u; o[1] = 0x00003F80u; }
      const int blk = tok >> 5, hh = (d & 15) >> 3, j = d & 7;
      *(u32x2*)(Kf + ((size_t)((blk * 2 + t) * 64 + (tok & 31) + 32 * hh)) * 8 + j) = o;
    }
    float mbk[4], rsk[4];
#pragma unroll
    for (int r = 0; r < 4; ++r) { mbk[r] = __shfl(mb, 4 * g + r); rsk[r] = __shfl(rs, 4 * g + r); }
#pragma unroll
    for (int t = 2; t < 4; ++t) {
      const int d = 16 * (t - 2) + i16;
      const float cs = csb[16 * t + i16];
      u32x2 o;
      o[0] = pack2((acc[t][0] - mbk[0] * cs) * rsk[0], (acc[t][1] - mbk[1] * cs) * rsk[1]);
      o[1] = pack2((acc[t][2] - mbk[2] * cs) * rsk[2], (acc[t][3] - mbk[3] * cs) * rsk[3]);
      if (d == 28) { o[0] = 0x3F803F80u; o[1] = 0x3F803F80u; }
      const int blk = tb * 2 + (w >> 1), s = w & 1;
      *(u32x2*)(Vf + ((size_t)((blk * 2 + s) * 64 + d + 32 * (g & 1))) * 8 + 4 * (g >> 1)) = o;
    }
  }
  if (do_wo) {
    const int t = fi / 14, s = fi % 14;
    u32x4 v;
#pragma unroll
    for (int j = 0; j < 4; ++j) v[j] = wov[2 * j] | (wov[2 * j + 1] << 16);
    const int dst = ((t / 7) * 2 + s / 7) * 49 + (s % 7) * 7 + t % 7;
    *(u32x4*)(WoF + ((size_t)dst * 64 + l) * 8) = v;
  }
  STAMP(0, 3);
}

constexpr int PART_ROW = 144;
constexpr int PART_W = 128 * PART_ROW;
constexpr int K2_OBUF = 8 * PART_W;
constexpr int K2_DUMMY = K2_OBUF + 8 * D_ * 2;
constexpr int K2_SMEM = K2_DUMMY + 256;

DI float wave_sum(float v) {
  v += __builtin_bit_cast(float, __builtin_amdgcn_update_dpp(0, __builtin_bit_cast(int, v), 0x111, 0xf, 0xf, false));
  v += __builtin_bit_cast(float, __builtin_amdgcn_update_dpp(0, __builtin_bit_cast(int, v), 0x112, 0xf, 0xf, false));
  v += __builtin_bit_cast(float, __builtin_amdgcn_update_dpp(0, __builtin_bit_cast(int, v), 0x114, 0xf, 0xf, false));
  v += __builtin_bit_cast(float, __builtin_amdgcn_update_dpp(0, __builtin_bit_cast(int, v), 0x118, 0xf, 0xf, false));
  v += __builtin_bit_cast(float, __builtin_amdgcn_update_dpp(0, __builtin_bit_cast(int, v), 0x142, 0xa, 0xf, false));
  v += __builtin_bit_cast(float, __builtin_amdgcn_update_dpp(0, __builtin_bit_cast(int, v), 0x143, 0xc, 0xf, false));
  return __builtin_bit_cast(float, __builtin_amdgcn_readlane(__builtin_bit_cast(int, v), 63));
}

DI void attn_phase(char* smem, const u16* __restrict__ Q, const u16* __restrict__ Kf,
                   const u16* __restrict__ Vf, const u16* __restrict__ WoF, const u16* __restrict__ x,
                   u16* Hout, u16* HnF, const u16* __restrict__ w1, const u16* __restrict__ w2) {
  const int tid = threadIdx.x, l = tid & 63, h = l >> 5;
  const int w = __builtin_amdgcn_readfirstlane(tid >> 6);
  const int t0 = blockIdx.x * 8;

  STAMP(1, 0);
  if (w < 4) __builtin_amdgcn_s_setprio(2);
  const u32x4* kbase = (const u32x4*)Kf + (size_t)w * 8 * 128 + l;
  const u32x4* vbase = (const u32x4*)Vf + (size_t)w * 8 * 128 + l;
  u32x4 qf[4][2];
#pragma unroll
  for (int b = 0; b < 4; ++b)
#pragma unroll
    for (int s = 0; s < 2; ++s)
      qf[b][s] = *(const u32x4*)(Q + ((size_t)((blockIdx.x * 4 + b) * 2 + s) * 64 + l) * 8);

  const int role = (w + (blockIdx.x >> 3)) & 7;
  const int kh = role & 1, nq = role >> 1;
  const u32x4* wbase = (const u32x4*)WoF + (size_t)(role * 49) * 64 + l;
  constexpr int WO_EARLY = 0;
  u32x4 woe[WO_EARLY + 1];

  f32x16 zero16;
#pragma unroll
  for (int i = 0; i < 16; ++i) zero16[i] = 0.f;

  float mval[4];
  f32x16 o[4];
  int nmax = 1;
#pragma unroll
  for (int e = 0; e < WO_EARLY; ++e) woe[e] = wbase[e * 64];
  for (;;) {
    asm volatile("" : "+v"(kbase), "+v"(vbase));
    u32x4 k0n = kbase[0], k1n = kbase[64], v0n = vbase[0], v1n = vbase[64];
    u32x4 k0m = kbase[128], k1m = kbase[192], v0m = vbase[128], v1m = vbase[192];
    float mx[4];
#pragma unroll
    for (int b = 0; b < 4; ++b) {
      if (h) { qf[b][1][2] = 0u; qf[b][1][3] = 0u; }
      f32x16 s = MFMA32(asbf(k0n), asbf(qf[b][0]), zero16);
      s = MFMA32(asbf(k1n), asbf(qf[b][1]), s);
      float m = s[0];
#pragma unroll
      for (int i = 1; i < 16; ++i) m = fmaxf(m, s[i]);
      mx[b] = m;
    }
    for (int kb = 1; kb < nmax; ++kb) {
      const u32x4 k0 = kbase[kb * 128], k1 = kbase[kb * 128 + 64];
#pragma unroll
      for (int b = 0; b < 4; ++b) {
        f32x16 s = MFMA32(asbf(k0), asbf(qf[b][0]), zero16);
        s = MFMA32(asbf(k1), asbf(qf[b][1]), s);
#pragma unroll
        for (int i = 0; i < 16; ++i) mx[b] = fmaxf(mx[b], s[i]);
      }
    }
#pragma unroll
    for (int b = 0; b < 4; ++b) {
      const auto sw = __builtin_amdgcn_permlane32_swap(__float_as_uint(mx[b]), __float_as_uint(mx[b]), false, false);
      const float m = fmaxf(__uint_as_float(sw[0]), __uint_as_float(sw[1]));
      mval[b] = m;
      const float nm = -m;
      const unsigned hi = __float_as_uint(nm) & 0xffff0000u;
      const float r1 = nm - __uint_as_float(hi);
      const unsigned mid = __float_as_uint(r1) & 0xffff0000u;
      const float r2 = r1 - __uint_as_float(mid);
      const unsigned lo = __float_as_uint(r2) & 0xffff0000u;
      if (h) { qf[b][1][2] = (hi >> 16) | mid; qf[b][1][3] = lo >> 16; }
      o[b] = zero16;
    }
    STAMP(1, 1);
#define SB() __builtin_amdgcn_sched_barrier(0)
#define EXP4(S, i) do { S[i] = __builtin_amdgcn_exp2f(S[i]); S[i + 1] = __builtin_amdgcn_exp2f(S[i + 1]); \
                        S[i + 2] = __builtin_amdgcn_exp2f(S[i + 2]); S[i + 3] = __builtin_amdgcn_exp2f(S[i + 3]); } while (0)
#define STEP(SC, SN, B, BN, BP, KN0, KN1, V0, VP1) do { \
      u32x4 p0; \
      o[BP] = MFMA32(asbf(VP1), asbf(pp), o[BP]); SB(); \
      EXP4(SC, 0); SB(); \
      SN = MFMA32(asbf(KN0), asbf(qf[BN][0]), zero16); SB(); \
      EXP4(SC, 4); \
      p0[0] = pack2(SC[0], SC[1]); p0[1] = pack2(SC[2], SC[3]); p0[2] = pack2(SC[4], SC[5]); p0[3] = pack2(SC[6], SC[7]); SB(); \
      asm volatile("" :: "v"(pp)); \
      SN = MFMA32(asbf(KN1), asbf(qf[BN][1]), SN); SB(); \
      EXP4(SC, 8); SB(); \
      o[B] = MFMA32(asbf(V0), asbf(p0), o[B]); SB(); \
      EXP4(SC, 12); \
      pp[0] = pack2(SC[8], SC[9]); pp[1] = pack2(SC[10], SC[11]); pp[2] = pack2(SC[12], SC[13]); pp[3] = pack2(SC[14], SC[15]); SB(); \
      asm volatile("" :: "v"(p0)); \
    } while (0)
    {
      u32x4 kA0 = k0n, kA1 = k1n, vA0 = v0n, vA1 = v1n;
      u32x4 kB0 = k0m, kB1 = k1m, vB0 = v0m, vB1 = v1m;
      u32x4 kC0, kC1, vC0, vC1 = v1n;
      u32x4 pp = {0u, 0u, 0u, 0u};
      f32x16 sa, sb;
      sa = MFMA32(asbf(kA0), asbf(qf[0][0]), zero16);
      sa = MFMA32(asbf(kA1), asbf(qf[0][1]), sa);
#define BLOCK(KB, X, Y, Z, SA, SB_) do { \
        const int kn_ = ((KB) + 2) & 7; \
        k##Z##0 = kbase[kn_ * 128]; k##Z##1 = kbase[kn_ * 128 + 64]; v##Z##0 = vbase[kn_ * 128]; \
        SB(); \
        STEP(SA, SB_, 0, 1, 3, k##X##0, k##X##1, v##X##0, v##Z##1); \
        v##Z##1 = vbase[kn_ * 128 + 64]; \
        SB(); \
        STEP(SB_, SA, 1, 2, 0, k##X##0, k##X##1, v##X##0, v##X##1); \
        STEP(SA, SB_, 2, 3, 1, k##X##0, k##X##1, v##X##0, v##X##1); \
        STEP(SB_, SA, 3, 0, 2, k##Y##0, k##Y##1, v##X##0, v##X##1); \
      } while (0)
      auto pf_at = [&](int blk) {
        const bool j1 = blk == ((w * 3) & 7), j2 = (w == 1 && blk == 6);
        if (j1 || j2) {
          const int job = j1 ? w : 8;
          const int xcd = blockIdx.x & 7, bi = blockIdx.x >> 3;
          const u16* pf;
          if (job < 5) {
            const int idx = job * 64 + l, row = idx / 5, ln = idx - row * 5;
            int col = ((xcd * 49) >> 1) / 7 * 64 + ln * 64;
            col = col < F_ - 64 ? col : F_ - 64;
            pf = w1 + (size_t)(64 * bi + row) * F_ + col;
          } else {
            int idx = (job - 5) * 64 + l;
            idx = idx < 224 ? idx : 223;
            pf = w2 + (size_t)(56 * bi + (idx >> 2)) * S_ + 256 * xcd + (idx & 3) * 64;
          }
          touch(pf, (unsigned)(size_t)(smem + K2_DUMMY));
        }
      };
#pragma unroll 1
      for (int kb = 0; kb < 6; kb += 3) {
        BLOCK(kb, A, B, C, sa, sb);
        pf_at(kb);
        BLOCK(kb + 1, B, C, A, sa, sb);
        pf_at(kb + 1);
        BLOCK(kb + 2, C, A, B, sa, sb);
        pf_at(kb + 2);
      }
      BLOCK(6, A, B, C, sa, sb);
      pf_at(6);
      BLOCK(7, B, C, A, sa, sb);
      pf_at(7);
      o[3] = MFMA32(asbf(vB1), asbf(pp), o[3]);
    }
    bool ok = true;
#pragma unroll
    for (int b = 0; b < 4; ++b) ok = ok && (h == 0 || o[b][12] < 1.8446744e19f);
    if (nmax == 8 || __all(ok)) break;
    nmax = 8;
  }
  STAMP(1, 2);
  {
    char* pw = smem + w * PART_W;
#pragma unroll
    for (int b = 0; b < 4; ++b) {
      char* pr = pw + (32 * b + (l & 31)) * PART_ROW + 16 * h;
#pragma unroll
      for (int gq = 0; gq < 4; ++gq)
        *(f32x4*)(pr + 32 * gq) = (f32x4){o[b][4 * gq], o[b][4 * gq + 1], o[b][4 * gq + 2], o[b][4 * gq + 3]};
      if (h == 0) *(float*)(pr + 128) = mval[b];
    }
  }
  u32x4 xr;
  __builtin_amdgcn_s_setprio(0);
#define LOAD_WOF(S0, S1) do { \
    _Pragma("unroll") for (int s = S0; s < S1; ++s) \
      _Pragma("unroll") for (int i = 0; i < 7; ++i) \
        wof[s][i] = (s * 7 + i < WO_EARLY) ? woe[s * 7 + i] : wbase[(s * 7 + i) * 64]; \
  } while (0)
#define LOAD_XR() do { xr = *(const u32x4*)(x + (t0 + w) * D_ + 8 * (l < 56 ? l : 55)); } while (0)
#define WO_GEMM() do { \
    const int g = l >> 4; \
    f32x4 acc[7]; \
    _Pragma("unroll") for (int i = 0; i < 7; ++i) acc[i] = (f32x4){0.f, 0.f, 0.f, 0.f}; \
    _Pragma("unroll") for (int s = 0; s < 7; ++s) { \
      const u32x4 of = *(const u32x4*)(smem + K2_OBUF + (l & 7) * (D_ * 2) + 64 * (7 * kh + s) + 16 * g); \
      _Pragma("unroll") for (int i = 0; i < 7; ++i) acc[i] = MFMA16(asbf(wof[s][i]), asbf(of), acc[i]); \
    } \
    if ((l & 15) < 8) { \
      _Pragma("unroll") for (int i = 0; i < 7; ++i) \
        *(f32x4*)(smem + ((kh * 8 + (l & 15)) * D_ + 16 * (7 * nq + i) + 4 * g) * 4) = acc[i]; \
    } \
  } while (0)
  if (w < 4) {
    u32x4 wof[7][7];
    LOAD_WOF(0, 6);
    __builtin_amdgcn_sched_barrier(0);
    STAMP(1, 3);
    __syncthreads();
    STAMP(1, 4);
#pragma unroll 1
    for (int rep = 0; rep < 2; ++rep) {
      const int item = rep * 256 + tid;
      const int row = item >> 2, qd = item & 3;
      const char* pr0 = smem + row * PART_ROW;
      float M = -3.0e38f;
#pragma unroll
      for (int ww = 0; ww < 8; ++ww) M = fmaxf(M, *(const float*)(pr0 + ww * PART_W + 128));
      float lsum = 0.f;
      f32x4 a0 = {0.f, 0.f, 0.f, 0.f}, a1 = {0.f, 0.f, 0.f, 0.f};
#pragma unroll
      for (int ww = 0; ww < 8; ++ww) {
        const char* pr = pr0 + ww * PART_W;
        const float e = __builtin_amdgcn_exp2f(*(const float*)(pr + 128) - M);
        lsum += e * *(const float*)(pr + 112);
        a0 += e * *(const f32x4*)(pr + 32 * qd);
        a1 += e * *(const f32x4*)(pr + 32 * qd + 16);
      }
      const float inv = __builtin_amdgcn_rcpf(lsum);
      char* ob = smem + K2_OBUF + (row >> 4) * (D_ * 2) + (row & 15) * (DH_ * 2) + 16 * qd;
      u32x2 o0, o1;
      o0[0] = pack2(a0[0] * inv, a0[1] * inv); o0[1] = pack2(a0[2] * inv, a0[3] * inv);
      o1[0] = pack2(a1[0] * inv, a1[1] * inv); o1[1] = pack2(a1[2] * inv, a1[3] * inv);
      *(u32x2*)ob = o0;
      if (qd < 3) *(u32x2*)(ob + 8) = o1;
    }
    LOAD_WOF(6, 7);
    LOAD_XR();
    __builtin_amdgcn_sched_barrier(0);
    STAMP(1, 5);
    __syncthreads();
    STAMP(1, 6);
    WO_GEMM();
  } else {
    STAMP(1, 3);
    __syncthreads();
    STAMP(1, 4);
    u32x4 wof[7][7];
    LOAD_WOF(0, 7);
    LOAD_XR();
    __builtin_amdgcn_sched_barrier(0);
    STAMP(1, 5);
    __syncthreads();
    STAMP(1, 6);
    WO_GEMM();
  }
  STAMP(1, 7);
  __syncthreads();
  STAMP(1, 8);
  {
    const int lc = l < 56 ? l : 55;
    const float live = l < 56 ? 1.0f : 0.0f;
    const char* r0 = smem + (w * D_ + 8 * lc) * 4;
    const f32x4 a0 = *(const f32x4*)r0 + *(const f32x4*)(r0 + 8 * D_ * 4);
    const f32x4 a1 = *(const f32x4*)(r0 + 16) + *(const f32x4*)(r0 + 8 * D_ * 4 + 16);
    u32x4 hv;
    float v[8];
#pragma unroll
    for (int j = 0; j < 4; ++j) {
      const unsigned ab = j < 2 ? pack2(a0[2 * j], a0[2 * j + 1]) : pack2(a1[2 * j - 4], a1[2 * j - 3]);
      hv[j] = pack2(bflo(xr[j]) + bflo(ab), bfhi(xr[j]) + bfhi(ab));
      v[2 * j] = bflo(hv[j]); v[2 * j + 1] = bfhi(hv[j]);
    }
    if (l < 56) store_sc1(Hout + (t0 + w) * D_ + 8 * l, hv);
#pragma unroll
    for (int rep = 0; rep < 2; ++rep) {
      const float c = __builtin_bit_cast(float, __builtin_amdgcn_readfirstlane(__builtin_bit_cast(int, v[0])));
      float s1 = 0.f, s2 = 0.f;
#pragma unroll
      for (int i = 0; i < 8; ++i) { const float a = v[i] - c; s1 += a; s2 = fmaf(a, a, s2); }
      s1 = wave_sum(s1 * live) * (1.0f / D_);
      s2 = wave_sum(s2 * live) * (1.0f / D_);
      const float mean = c + s1;
      const float var = fmaxf(s2 - s1 * s1, 0.f);
      const float mb = bfr(mean);
      const float rs = __builtin_amdgcn_rcpf(bfr(__builtin_amdgcn_sqrtf(bfr(bfr(var) + 1e-5f))));
      const float nb = -mb * rs;
#pragma unroll
      for (int j = 0; j < 4; ++j) {
        hv[j] = pack2(fmaf(v[2 * j], rs, nb), fmaf(v[2 * j + 1], rs, nb));
        v[2 * j] = bflo(hv[j]); v[2 * j + 1] = bfhi(hv[j]);
      }
    }
    if (l < 56) *(u32x4*)(smem + K2_OBUF + (w * D_ + 8 * l) * 2) = hv;
  }
  STAMP(1, 9);
  __syncthreads();
  STAMP(1, 10);
  if (tid < D_) {
    const u16* hb = (const u16*)(smem + K2_OBUF);
    u32x4 v;
#pragma unroll
    for (int j = 0; j < 4; ++j)
      v[j] = (unsigned)hb[(2 * j) * D_ + tid] | ((unsigned)hb[(2 * j + 1) * D_ + tid] << 16);
    const int lane = ((t0 >> 3) & 3) * 16 + (tid & 15);
    store_sc1(HnF + ((size_t)((((tid >> 6) * 64 + (t0 >> 5)) * 4 + ((tid >> 4) & 3)) * 64 + lane)) * 8, v);
  }
  STAMP(1, 11);
}

constexpr int G_SMEM = 8 * 16384;
constexpr int GDEPTH = 4;

template <int MODE>
DI void gemm_phase(char* smem, int dt, int nt, const u16* __restrict__ W, const u16* act,
                   const u16* __restrict__ bias, const u16* Hres, u16* out,
                   unsigned* wait_cnt, int wait_n, unsigned wait_target) {
  constexpr int LDW = MODE == 0 ? F_ : S_;
  constexpr int NKS = MODE == 0 ? S_ / 32 : F_ / 32;
  constexpr int KPW = NKS / 8;
  const int tid = threadIdx.x, l = tid & 63;
  const int w = __builtin_amdgcn_readfirstlane(tid >> 6);
  char* ring = smem + w * (GDEPTH * 4096);
  const int g = l >> 4, i16 = l & 15, q4 = i16 >> 2, p4 = l & 3;
  const u16* asrc = act + ((size_t)dt * NKS + w * KPW) * 2048 + l * 8;
  const int fsrc = (l >> 4) & 1;
  const u16* wsrc = W + (size_t)(w * KPW * 32 + (l >> 3)) * LDW + 64 * nt + (l & 1) * 8;
  const int c0 = (l & 7) >> 1;
  const int ecol = tid >> 3, er8 = (tid & 7) * 8;
  const unsigned ring_a = (unsigned)(size_t)ring;

  u32x4 ar[GDEPTH][4];
  auto issueW = [&](int i) {
#pragma unroll
    for (int j = 0; j < 4; ++j) {
      const int c = c0 ^ (fsrc | ((j & 1) << 1));
      unsigned keep;
      asm volatile("s_mov_b32 %0, m0\n\ts_mov_b32 m0, %2\n\ts_nop 0\n\tglobal_load_lds_dwordx4 %1, off\n\ts_mov_b32 m0, %0"
                   : "=&s"(keep) : "v"(wsrc + (size_t)(i * 32 + 8 * j) * LDW + c * 16),
                     "s"(ring_a + (i % GDEPTH) * 4096 + j * 1024) : "memory");
    }
  };
  auto issueA = [&](int i) {
#pragma unroll
    for (int j = 0; j < 4; ++j)
      asm volatile("global_load_dwordx4 %0, %1, off sc1" : "=v"(ar[i % GDEPTH][j]) : "v"(asrc + (size_t)(i * 4 + j) * 512) : "memory");
  };

  const int fl = ((q4 >> 1) & 1) | ((g & 1) << 1);
  unsigned trb[4];
#pragma unroll
  for (int t = 0; t < 4; ++t) trb[t] = ring_a + (8 * g + q4) * 128 + p4 * 8 + ((t ^ fl) * 32);
#define G_TRREAD(DST, SL, N, ...) asm volatile( \
          "s_waitcnt vmcnt(%c10)\n\t" \
          "ds_read_b64_tr_b16 %0, %11 offset:%c8\n\t" \
          "ds_read_b64_tr_b16 %1, %11 offset:%c9\n\t" \
          "ds_read_b64_tr_b16 %2, %12 offset:%c8\n\t" \
          "ds_read_b64_tr_b16 %3, %12 offset:%c9\n\t" \
          "ds_read_b64_tr_b16 %4, %13 offset:%c8\n\t" \
          "ds_read_b64_tr_b16 %5, %13 offset:%c9\n\t" \
          "ds_read_b64_tr_b16 %6, %14 offset:%c8\n\t" \
          "ds_read_b64_tr_b16 %7, %14 offset:%c9\n\t" \
          "s_waitcnt lgkmcnt(0)" \
          : "=&v"(DST[0]), "=&v"(DST[1]), "=&v"(DST[2]), "=&v"(DST[3]), "=&v"(DST[4]), "=&v"(DST[5]), "=&v"(DST[6]), "=&v"(DST[7]) \
          : "i"((SL) * 4096), "i"((SL) * 4096 + 512), "i"(N), "v"(trb[0]), "v"(trb[1]), "v"(trb[2]), "v"(trb[3]) : "memory")
#define G_WF(R, t) asbf((u32x4){R[2 * (t)][0], R[2 * (t)][1], R[2 * (t) + 1][0], R[2 * (t) + 1][1]})

  STAMP(2 + MODE, 0);
  u32x2 wreg[GDEPTH][8];
#pragma unroll
  for (int i = 0; i < GDEPTH; ++i) issueW(i);
#pragma unroll
  for (int i = 0; i < GDEPTH; ++i) {
    G_TRREAD(wreg[i], i, 4 * (GDEPTH - 1 - i) + 4 * ((i + GDEPTH <= KPW ? i : KPW - GDEPTH)));
    if (i + GDEPTH < KPW) issueW(i + GDEPTH);
  }
  if (w == 0) wait_counters(wait_cnt, wait_n, wait_target, l);
  asm volatile("s_waitcnt lgkmcnt(0)" ::: "memory");
  __builtin_amdgcn_s_barrier();
  asm volatile("" ::: "memory");
  STAMP(2 + MODE, 1);
#pragma unroll
  for (int i = 0; i < GDEPTH; ++i) issueA(i);

  f32x4 acc[4][4];
#pragma unroll
  for (int a = 0; a < 4; ++a)
#pragma unroll
    for (int b = 0; b < 4; ++b) acc[a][b] = (f32x4){0.f, 0.f, 0.f, 0.f};

  u32x4 ep;
  unsigned epb = 0;
#pragma unroll
  for (int i = 0; i < KPW; ++i) {
    const int full = (KPW - 1 - i) < (GDEPTH - 1) ? (KPW - 1 - i) : (GDEPTH - 1);
    const int extra = (i == KPW - 1) ? (MODE == 0 ? 1 : 2) : 0;
    const int pend = full * 4 + extra;
    if (i == KPW - 1) {
      if (MODE == 0) {
        asm volatile("global_load_dwordx4 %0, %1, off" : "=v"(ep) : "v"(bias + 64 * nt + er8) : "memory");
      } else {
        asm volatile("global_load_dwordx4 %0, %1, off sc1" : "=v"(ep) : "v"(Hres + (size_t)(64 * nt + ecol) * D_ + 64 * dt + er8) : "memory");
        asm volatile("global_load_ushort %0, %1, off" : "=v"(epb) : "v"(bias + 64 * nt + ecol) : "memory");
      }
    }
    u32x4 a0 = ar[i % GDEPTH][0], a1 = ar[i % GDEPTH][1], a2 = ar[i % GDEPTH][2], a3 = ar[i % GDEPTH][3];
    u32x2 wl[8];
    if (i >= GDEPTH) G_TRREAD(wl, i - GDEPTH, 63);
    asm volatile("s_waitcnt vmcnt(%c4)" : "+v"(a0), "+v"(a1), "+v"(a2), "+v"(a3) : "i"(pend) : "memory");
    if (i + GDEPTH < KPW) issueA(i + GDEPTH);
    const bf16x8 af[4] = {asbf(a0), asbf(a1), asbf(a2), asbf(a3)};
    bf16x8 wf[4];
#pragma unroll
    for (int t = 0; t < 4; ++t) wf[t] = i < GDEPTH ? G_WF(wreg[i % GDEPTH], t) : G_WF(wl, t);
#pragma unroll
    for (int a = 0; a < 4; ++a)
#pragma unroll
      for (int b = 0; b < 4; ++b) {
        if (MODE == 0) acc[a][b] = MFMA16(wf[a], af[b], acc[a][b]);
        else           acc[a][b] = MFMA16(af[a], wf[b], acc[a][b]);
      }
  }
  STAMP(2 + MODE, 2);
  {
#pragma unroll
    for (int a = 0; a < 4; ++a)
#pragma unroll
      for (int b = 0; b < 4; ++b)
        *(f32x4*)(ring + (16 * b + i16) * 256 + (((4 * a + g) ^ i16) * 16)) = acc[a][b];
  }
  asm volatile("s_waitcnt vmcnt(0)" : "+v"(ep), "+v"(epb) :: "memory");
  STAMP(2 + MODE, 3);
  __syncthreads();
  {
    f32x4 s0 = {0.f, 0.f, 0.f, 0.f}, s1 = {0.f, 0.f, 0.f, 0.f};
    const int ch = (tid & 7) * 2;
    const char* p0 = smem + ecol * 256 + ((ch ^ (ecol & 15)) * 16);
    const char* p1 = smem + ecol * 256 + (((ch + 1) ^ (ecol & 15)) * 16);
#pragma unroll
    for (int ww = 0; ww < 8; ++ww) {
      s0 += *(const f32x4*)(p0 + ww * 16384);
      s1 += *(const f32x4*)(p1 + ww * 16384);
    }
    float v[8] = {s0[0], s0[1], s0[2], s0[3], s1[0], s1[1], s1[2], s1[3]};
    u32x4 ov;
    if (MODE == 0) {
      const int d = 64 * dt + ecol, f = 64 * nt + er8;
#pragma unroll
      for (int j = 0; j < 8; ++j) {
        const float bb = (j & 1) ? bfhi(ep[j >> 1]) : bflo(ep[j >> 1]);
        const float t = bfr(bfr(v[j]) + bb);
        const float y = 0.7978845608028654f * (t + 0.044715f * t * t * t);
        v[j] = t * __builtin_amdgcn_rcpf(1.0f + __expf(-2.0f * y));
      }
#pragma unroll
      for (int j = 0; j < 4; ++j) ov[j] = pack2(v[2 * j], v[2 * j + 1]);
      const int lane = ((f & 31) >> 3) * 16 + (d & 15);
      store_sc1(out + ((size_t)(((d >> 6) * (F_ / 32) + (f >> 5)) * 4 + ((d >> 4) & 3)) * 64 + lane) * 8, ov);
    } else {
      const int s = 64 * nt + ecol, d = 64 * dt + er8;
      const float bb = bf2f(epb);
#pragma unroll
      for (int j = 0; j < 8; ++j) {
        const float hh = (j & 1) ? bfhi(ep[j >> 1]) : bflo(ep[j >> 1]);
        v[j] = hh + bfr(bfr(v[j]) + bb);
      }
#pragma unroll
      for (int j = 0; j < 4; ++j) ov[j] = pack2(v[2 * j], v[2 * j + 1]);
      *(u32x4*)(out + (size_t)s * D_ + d) = ov;
    }
  }
  STAMP(2 + MODE, 4);
}

__global__ __launch_bounds__(512, 2) void k_fused(const u16* __restrict__ Q, const u16* __restrict__ Kf,
                                                  const u16* __restrict__ Vf, const u16* __restrict__ WoF,
                                                  const u16* __restrict__ x, u16* Hb, u16* HnF, u16* UF,
                                                  const u16* __restrict__ w1, const u16* __restrict__ b1,
                                                  const u16* __restrict__ w2, const u16* __restrict__ b2,
                                                  u16* out, unsigned* cnt) {
  extern __shared__ __attribute__((aligned(16))) char smem[];
  const int tid = threadIdx.x;
  const int xcd = blockIdx.x & 7, bi = blockIdx.x >> 3;
  unsigned* cntA = cnt;
  unsigned* cntB = cnt + NCNT_A * CNT_STRIDE;

  attn_phase(smem, Q, Kf, Vf, WoF, x, Hb, HnF, w1, w2);
  asm volatile("s_waitcnt vmcnt(0)" ::: "memory");
  __syncthreads();
  if (tid == 0) signal_counter(cntA + (blockIdx.x & (NCNT_A - 1)) * CNT_STRIDE);

  {
    const int start = (xcd * 49) >> 1, cntx = ((xcd + 1) * 49 >> 1) - start;
    if (bi < cntx) {
      const int j = start + bi, nt = j / 7, dt = j - nt * 7;
      gemm_phase<0>(smem, dt, nt, w1, HnF, b1, nullptr, UF, cntA, NCNT_A, 256 / NCNT_A);
      asm volatile("s_waitcnt vmcnt(0)" ::: "memory");
      __syncthreads();
      if (tid == 0) signal_counter(cntB + dt * CNT_STRIDE);
    }
  }
  if (bi < 28) {
    const int j = xcd * 28 + bi, nt = j / 7, dt = j - nt * 7;
    gemm_phase<1>(smem, dt, nt, w2, UF, b2, Hb, out, cntB + dt * CNT_STRIDE, 1, 28);
  }
}

extern "C" void kernel_launch(void* const* d_in, const int* in_sizes, int n_in, void* d_out, int out_size,
                              void* d_ws, size_t ws_size, hipStream_t stream) {
  const u16* x  = (const u16*)d_in[0];
  const u16* wq = (const u16*)d_in[1];
  const u16* wk = (const u16*)d_in[2];
  const u16* wv = (const u16*)d_in[3];
  const u16* wo = (const u16*)d_in[4];
  const u16* w1 = (const u16*)d_in[6];
  const u16* b1 = (const u16*)d_in[7];
  const u16* w2 = (const u16*)d_in[8];
  const u16* b2 = (const u16*)d_in[9];
  char* ws = (char*)d_ws;
  u16* Q   = (u16*)(ws + OFF_Q);
  u16* Kf  = (u16*)(ws + OFF_KF);
  u16* Vf  = (u16*)(ws + OFF_VF);
  u16* WoF = (u16*)(ws + OFF_WOF);
  u16* Hb  = (u16*)(ws + OFF_H);
  u16* HnF = (u16*)(ws + OFF_HNF);
  u16* UF  = (u16*)(ws + OFF_UF);

  unsigned* cnt = (unsigned*)(ws + OFF_CNT);

  (void)hipFuncSetAttribute((const void*)k_fused, hipFuncAttributeMaxDynamicSharedMemorySize, K2_SMEM);

  k1_ln_qkv<<<256, 256, 0, stream>>>(x, wq, wk, wv, wo, Q, Kf, Vf, WoF, cnt);
  k_fused<<<256, 512, K2_SMEM, stream>>>(Q, Kf, Vf, WoF, x, Hb, HnF, UF, w1, b1, w2, b2, (u16*)d_out, cnt);
}
```

```cpp
#include <hip/hip_runtime.h>
#include <hip/hip_bf16.h>
#include <stdint.h>

typedef unsigned short u16;
typedef __attribute__((ext_vector_type(8))) short bf16x8;
typedef __attribute__((ext_vector_type(4))) short s16x4;
typedef __attribute__((ext_vector_type(4))) float f32x4;
typedef __attribute__((ext_vector_type(16))) float f32x16;
typedef __attribute__((ext_vector_type(4))) unsigned u32x4;
typedef __attribute__((ext_vector_type(2))) unsigned u32x2;

#define DI __device__ __forceinline__
#define MFMA32(a, b, c) __builtin_amdgcn_mfma_f32_32x32x16_bf16((a), (b), (c), 0, 0, 0)
#define MFMA16(a, b, c) __builtin_amdgcn_mfma_f32_16x16x32_bf16((a), (b), (c), 0, 0, 0)

constexpr int S_ = 2048, D_ = 448, H_ = 16, DH_ = 28, F_ = 1792;

constexpr size_t OFF_Q   = 0;
constexpr size_t OFF_KF  = OFF_Q + (size_t)S_ * H_ * 32 * 2;
constexpr size_t OFF_VF  = OFF_KF + (size_t)S_ * 32 * 2;
constexpr size_t OFF_WOF = OFF_VF + (size_t)S_ * 32 * 2;
constexpr size_t OFF_H   = OFF_WOF + (size_t)D_ * D_ * 2;
constexpr size_t OFF_HNF = OFF_H + (size_t)S_ * D_ * 2;
constexpr size_t OFF_UF  = OFF_HNF + (size_t)S_ * D_ * 2;
constexpr size_t OFF_CNT = OFF_UF + (size_t)F_ * D_ * 2;

DI float bf2f(unsigned v) { return __uint_as_float(v << 16); }
DI float bflo(unsigned v) { return __uint_as_float(v << 16); }
DI float bfhi(unsigned v) { return __uint_as_float(v & 0xffff0000u); }
DI unsigned pack2(float a, float b) {
  typedef __attribute__((ext_vector_type(2))) __bf16 bf2;
  typedef __attribute__((ext_vector_type(2))) float f2;
  f2 v = {a, b};
  bf2 r = __builtin_convertvector(v, bf2);
  return __builtin_bit_cast(unsigned, r);
}
DI float bfr(float a) { return bflo(pack2(a, 0.f)); }
DI bf16x8 asbf(u32x4 v) { return __builtin_bit_cast(bf16x8, v); }

DI void glds16(const void* g, void* l) {
  __builtin_amdgcn_global_load_lds((const unsigned*)g, (unsigned*)l, 16, 0, 0);
}
DI void touch(const void* g, unsigned lds_dummy) {
  unsigned keep;
  asm volatile("s_mov_b32 %0, m0\n\ts_mov_b32 m0, %2\n\ts_nop 0\n\tglobal_load_lds_dword %1, off\n\ts_mov_b32 m0, %0"
               : "=&s"(keep) : "v"(g), "s"(lds_dummy) : "memory");
}
DI void store_sc1(void* g, u32x4 v) {
  asm volatile("global_store_dwordx4 %0, %1, off sc1\n\ts_nop 1" :: "v"(g), "v"(v) : "memory");
}
constexpr int NCNT_A = 16;
constexpr int CNT_STRIDE = 32;
DI void signal_counter(unsigned* c) {
  __hip_atomic_fetch_add(c, 1u, __ATOMIC_RELAXED, __HIP_MEMORY_SCOPE_AGENT);
}
DI void wait_counters(unsigned* c, int n, unsigned target, int l) {
  unsigned* p = c + (l < n ? l : 0) * CNT_STRIDE;
  for (int spin = 0; spin < 4000000; ++spin) {
    const unsigned v = __hip_atomic_load(p, __ATOMIC_RELAXED, __HIP_MEMORY_SCOPE_AGENT);
    if (__all(v >= target)) break;
    __builtin_amdgcn_s_sleep(1);
  }
}
DI s16x4 tr_read(const char* p) {
  return __builtin_amdgcn_ds_read_tr16_b64_v4i16((__attribute__((address_space(3))) s16x4*)(p));
}
#define STAMP(k, i) do {} while (0)
DI bf16x8 cat(s16x4 lo, s16x4 hi) { return __builtin_shufflevector(lo, hi, 0, 1, 2, 3, 4, 5, 6, 7); }

__global__ __launch_bounds__(256) void k1_ln_qkv(const u16* __restrict__ x, const u16* __restrict__ wq,
                                                 const u16* __restrict__ wk, const u16* __restrict__ wv,
                                                 const u16* __restrict__ wo, u16* __restrict__ Q,
                                                 u16* __restrict__ Kf, u16* __restrict__ Vf,
                                                 u16* __restrict__ WoF, unsigned* __restrict__ cnt) {
  __shared__ __attribute__((aligned(16))) char smem[2 * D_ * 128];
  const int tid = threadIdx.x, l = tid & 63;
  const int w = __builtin_amdgcn_readfirstlane(tid >> 6);
  const int bx = blockIdx.x, xcd = bx & 7, ii = bx >> 3;
  const int tb = xcd + 8 * (ii >> 3), nt = ii & 7;
  const int g = l >> 4, i16 = l & 15;

  STAMP(0, 0);
  if (bx == 0 && tid < NCNT_A + 7) cnt[tid * CNT_STRIDE] = 0u;
  unsigned wov[8];
  const bool do_wo = w < 2 && bx < 196;
  const int fi = do_wo ? 2 * bx + w : 0;
  {
    const u16* src = wo + (32 * (fi % 14) + 8 * g) * D_ + 16 * (fi / 14) + i16;
#pragma unroll
    for (int j = 0; j < 8; ++j)
      asm volatile("global_load_ushort %0, %1, off offset:%c2" : "=v"(wov[j]) : "v"(src + (j >> 2) * 4 * D_), "i"((j & 3) * D_ * 2) : "memory");
  }
  {
    const u16* xs = x + (size_t)(tb * 64 + w * 16) * D_;
    int row = l >= 56 ? 1 : 0, pos = l >= 56 ? l - 56 : l;
#pragma unroll
    for (int it = 0; it < 14; ++it) {
      glds16(xs + row * D_ + ((pos & ~7) | ((pos ^ row) & 7)) * 8, smem + D_ * 128 + w * 14336 + it * 1024);
      pos += 8; row += 1;
      if (pos >= 56) { pos -= 56; row += 1; }
    }
  }
  if (nt < 7) {
#pragma unroll
    for (int it = 0; it < 14; ++it) {
      const int piece = it * 4 + w;
      const int r = piece * 8 + (l >> 3);
      const int fr = ((r >> 1) & 1) | (((r >> 3) & 1) << 1);
      const int c = ((l & 7) >> 1) ^ fr;
      glds16(wq + r * D_ + nt * 64 + (2 * c + (l & 1)) * 8, smem + piece * 1024);
    }
  } else {
    const int col = (tid & 31) * 2, c16 = col & 31;
    const bool valid = c16 < DH_;
    const u16* wsrc = (col < 32 ? wk : wv) + (valid ? c16 : 0) + (tid >> 5) * DH_;
    unsigned v[56];
#pragma unroll
    for (int it = 0; it < 56; ++it) v[it] = *(const unsigned*)(wsrc + it * 8 * DH_);
#pragma unroll
    for (int it = 0; it < 56; ++it) {
      const int r = (tid >> 5) + 8 * it;
      const int fr = ((r >> 1) & 1) | (((r >> 3) & 1) << 1);
      *(unsigned*)(smem + r * 128 + (((col >> 4) ^ fr) * 32) + (col & 15) * 2) = valid ? v[it] : 0u;
    }
  }

  const int tok = tb * 64 + w * 16 + i16;
  STAMP(0, 1);
  asm volatile("s_waitcnt vmcnt(0)" : "+v"(wov[0]), "+v"(wov[1]), "+v"(wov[2]), "+v"(wov[3]), "+v"(wov[4]), "+v"(wov[5]),
               "+v"(wov[6]), "+v"(wov[7]) :: "memory");
  __syncthreads();
  STAMP(0, 2);
  u32x4 xf[14];
#pragma unroll
  for (int s = 0; s < 14; ++s) {
    const int c = 4 * s + g;
    xf[s] = *(const u32x4*)(smem + D_ * 128 + w * 14336 + i16 * 896 + ((c & ~7) | ((c ^ i16) & 7)) * 16);
  }
  const u32x4 ones = {0x3F803F80u, 0x3F803F80u, 0x3F803F80u, 0x3F803F80u};
  f32x4 accS = {0.f, 0.f, 0.f, 0.f}, accG = {0.f, 0.f, 0.f, 0.f};
  asm volatile("s_waitcnt lgkmcnt(0)" : "+v"(xf[0]), "+v"(xf[1]), "+v"(xf[2]), "+v"(xf[3]), "+v"(xf[4]), "+v"(xf[5]), "+v"(xf[6]),
               "+v"(xf[7]), "+v"(xf[8]), "+v"(xf[9]), "+v"(xf[10]), "+v"(xf[11]), "+v"(xf[12]), "+v"(xf[13]) :: "memory");

  const int q4 = i16 >> 2, p4 = l & 3;
  const int fl = ((q4 >> 1) & 1) | ((g & 1) << 1);
  unsigned trb[4];
#pragma unroll
  for (int t = 0; t < 4; ++t) trb[t] = (unsigned)(size_t)smem + (8 * g + q4) * 128 + p4 * 8 + ((t ^ fl) * 32);
  f32x4 acc[4], accC = {0.f, 0.f, 0.f, 0.f};
#pragma unroll
  for (int t = 0; t < 4; ++t) acc[t] = (f32x4){0.f, 0.f, 0.f, 0.f};
  u32x2 wr[3][8];
#define K1_RD7(S) asm volatile( \
          "ds_read_b64_tr_b16 %0, %7 offset:%c11\n\t" \
          "ds_read_b64_tr_b16 %1, %7 offset:%c11+512\n\t" \
          "ds_read_b64_tr_b16 %2, %8 offset:%c11\n\t" \
          "ds_read_b64_tr_b16 %3, %8 offset:%c11+512\n\t" \
          "ds_read_b64_tr_b16 %4, %9 offset:%c11\n\t" \
          "ds_read_b64_tr_b16 %5, %9 offset:%c11+512\n\t" \
          "ds_read_b64_tr_b16 %6, %10 offset:%c11" \
          : "=&v"(wr[(S) % 3][0]), "=&v"(wr[(S) % 3][1]), "=&v"(wr[(S) % 3][2]), "=&v"(wr[(S) % 3][3]), \
            "=&v"(wr[(S) % 3][4]), "=&v"(wr[(S) % 3][5]), "=&v"(wr[(S) % 3][6]) \
          : "v"(trb[0]), "v"(trb[1]), "v"(trb[2]), "v"(trb[3]), "i"((S) * 4096) : "memory")
#define K1_RD8(S) asm volatile("ds_read_b64_tr_b16 %0, %1 offset:%c2" : "=&v"(wr[(S) % 3][7]) : "v"(trb[3]), "i"((S) * 4096 + 512) : "memory")
#define K1_WAIT(S, N) asm volatile("s_waitcnt lgkmcnt(%c8)" \
          : "+v"(wr[(S) % 3][0]), "+v"(wr[(S) % 3][1]), "+v"(wr[(S) % 3][2]), "+v"(wr[(S) % 3][3]), \
            "+v"(wr[(S) % 3][4]), "+v"(wr[(S) % 3][5]), "+v"(wr[(S) % 3][6]), "+v"(wr[(S) % 3][7]) : "i"(N) : "memory")
#define K1_WF(S, t) asbf((u32x4){wr[(S) % 3][2 * (t)][0], wr[(S) % 3][2 * (t)][1], wr[(S) % 3][2 * (t) + 1][0], wr[(S) % 3][2 * (t) + 1][1]})
#define K1_LOOP(KV) do { \
    _Pragma("unroll") for (int s = 0; s < 14; ++s) { \
      if (s < 12) { K1_RD7(s + 2); K1_WAIT(s, 15); K1_RD8(s + 2); } \
      else if (s == 12) { K1_WAIT(s, 8); } \
      else { K1_WAIT(s, 0); } \
      accS = MFMA16(asbf(ones), asbf(xf[s]), accS);        \
      accG = MFMA16(asbf(xf[s]), asbf(xf[s]), accG);       \
      acc[0] = MFMA16(K1_WF(s, 0), asbf(xf[s]), acc[0]); \
      acc[1] = MFMA16(K1_WF(s, 1), asbf(xf[s]), acc[1]); \
      if (!(KV)) { \
        acc[2] = MFMA16(K1_WF(s, 2), asbf(xf[s]), acc[2]);        \
        acc[3] = MFMA16(K1_WF(s, 3), asbf(xf[s]), acc[3]); \
      } else { \
        acc[2] = MFMA16(asbf(xf[s]), K1_WF(s, 2), acc[2]);        \
        acc[3] = MFMA16(asbf(xf[s]), K1_WF(s, 3), acc[3]); \
      } \
    } } while (0)
  K1_RD7(0); K1_RD8(0); K1_RD7(1); K1_RD8(1);
  const bool kv = nt == 7;
  if (!kv) K1_LOOP(false); else K1_LOOP(true);
  {
    const unsigned cb = (unsigned)(size_t)smem + (8 * g + q4) * 128 + p4 * 8 + ((w ^ fl) * 32);
    u32x2 cr[14][2];
#pragma unroll
    for (int hs = 0; hs < 2; ++hs) {
#define CS_(i) "ds_read_b64_tr_b16 %" #i ", %14 offset:%c15+"
      asm volatile(
          "ds_read_b64_tr_b16 %0, %14 offset:%c15\n\t"       "ds_read_b64_tr_b16 %1, %14 offset:%c15+512\n\t"
          "ds_read_b64_tr_b16 %2, %14 offset:%c15+4096\n\t"  "ds_read_b64_tr_b16 %3, %14 offset:%c15+4608\n\t"
          "ds_read_b64_tr_b16 %4, %14 offset:%c15+8192\n\t"  "ds_read_b64_tr_b16 %5, %14 offset:%c15+8704\n\t"
          "ds_read_b64_tr_b16 %6, %14 offset:%c15+12288\n\t" "ds_read_b64_tr_b16 %7, %14 offset:%c15+12800\n\t"
          "ds_read_b64_tr_b16 %8, %14 offset:%c15+16384\n\t" "ds_read_b64_tr_b16 %9, %14 offset:%c15+16896\n\t"
          "ds_read_b64_tr_b16 %10, %14 offset:%c15+20480\n\t" "ds_read_b64_tr_b16 %11, %14 offset:%c15+20992\n\t"
          "ds_read_b64_tr_b16 %12, %14 offset:%c15+24576\n\t" "ds_read_b64_tr_b16 %13, %14 offset:%c15+25088\n\t"
          "s_waitcnt lgkmcnt(0)"
          : "=&v"(cr[7 * hs][0]), "=&v"(cr[7 * hs][1]), "=&v"(cr[7 * hs + 1][0]), "=&v"(cr[7 * hs + 1][1]),
            "=&v"(cr[7 * hs + 2][0]), "=&v"(cr[7 * hs + 2][1]), "=&v"(cr[7 * hs + 3][0]), "=&v"(cr[7 * hs + 3][1]),
            "=&v"(cr[7 * hs + 4][0]), "=&v"(cr[7 * hs + 4][1]), "=&v"(cr[7 * hs + 5][0]), "=&v"(cr[7 * hs + 5][1]),
            "=&v"(cr[7 * hs + 6][0]), "=&v"(cr[7 * hs + 6][1])
          : "v"(cb), "i"(hs * 7 * 4096) : "memory");
#undef CS_
    }
#pragma unroll
    for (int s = 0; s < 14; ++s)
      accC = MFMA16(asbf(ones), asbf((u32x4){cr[s][0][0], cr[s][0][1], cr[s][1][0], cr[s][1][1]}), accC);
  }
  __syncthreads();
  float* csb = (float*)smem;
  if (g == 0) csb[16 * w + i16] = accC[0];
  float mb, rs;
  {
    const int r3 = l & 3;
    const float cand = r3 == 0 ? accG[0] : r3 == 1 ? accG[1] : r3 == 2 ? accG[2] : accG[3];
    const float g2 = __shfl(cand, i16 + 16 * (i16 >> 2));
    const float mean = accS[0] * (1.0f / D_);
    const float var = fmaxf(g2 * (1.0f / D_) - mean * mean, 0.f);
    mb = bfr(mean);
    rs = __builtin_amdgcn_rcpf(bfr(__builtin_amdgcn_sqrtf(bfr(bfr(var) + 1e-5f))));
  }
  __syncthreads();
  if (!kv) {
    const float qs = rs * (0.18898223650461362f * 1.4426950408889634f);
#pragma unroll
    for (int t = 0; t < 4; ++t) {
      const int n = nt * 64 + 16 * t + 4 * g;
      const int head = n / DH_, d = n - head * DH_;
      const f32x4 cs = *(const f32x4*)(csb + 16 * t + 4 * g);
      u32x2 o;
      o[0] = pack2((acc[t][0] - mb * cs[0]) * qs, (acc[t][1] - mb * cs[1]) * qs);
      o[1] = pack2((acc[t][2] - mb * cs[2]) * qs, (acc[t][3] - mb * cs[3]) * qs);
      const int r = (tok & 7) * 16 + head;
      *(u32x2*)(Q + ((size_t)((((tok >> 3) * 4 + (r >> 5)) * 2 + (d >> 4)) * 64 + (r & 31) + 32 * ((d >> 3) & 1))) * 8 + (d & 7)) = o;
    }
  } else {
#pragma unroll
    for (int t = 0; t < 2; ++t) {
      const int d = 16 * t + 4 * g;
      const f32x4 cs = *(const f32x4*)(csb + d);
      u32x2 o;
      o[0] = pack2((acc[t][0] - mb * cs[0]) * rs, (acc[t][1] - mb * cs[1]) * rs);
      o[1] = pack2((acc[t][2] - mb * cs[2]) * rs, (acc[t][3] - mb * cs[3]) * rs);
      if (d == 28) { o[0] = 0x3F803F80u; o[1] = 0x00003F80u; }
      const int blk = tok >> 5, hh = (d & 15) >> 3, j = d & 7;
      *(u32x2*)(Kf + ((size_t)((blk * 2 + t) * 64 + (tok & 31) + 32 * hh)) * 8 + j) = o;
    }
    float mbk[4], rsk[4];
#pragma unroll
    for (int r = 0; r < 4; ++r) { mbk[r] = __shfl(mb, 4 * g + r); rsk[r] = __shfl(rs, 4 * g + r); }
#pragma unroll
    for (int t = 2; t < 4; ++t) {
      const int d = 16 * (t - 2) + i16;
      const float cs = csb[16 * t + i16];
      u32x2 o;
      o[0] = pack2((acc[t][0] - mbk[0] * cs) * rsk[0], (acc[t][1] - mbk[1] * cs) * rsk[1]);
      o[1] = pack2((acc[t][2] - mbk[2] * cs) * rsk[2], (acc[t][3] - mbk[3] * cs) * rsk[3]);
      if (d == 28) { o[0] = 0x3F803F80u; o[1] = 0x3F803F80u; }
      const int blk = tb * 2 + (w >> 1), s = w & 1;
      *(u32x2*)(Vf + ((size_t)((blk * 2 + s) * 64 + d + 32 * (g & 1))) * 8 + 4 * (g >> 1)) = o;
    }
  }
  if (do_wo) {
    const int t = fi / 14, s = fi % 14;
    u32x4 v;
#pragma unroll
    for (int j = 0; j < 4; ++j) v[j] = wov[2 * j] | (wov[2 * j + 1] << 16);
    const int dst = ((t / 7) * 2 + s / 7) * 49 + (s % 7) * 7 + t % 7;
    *(u32x4*)(WoF + ((size_t)dst * 64 + l) * 8) = v;
  }
  STAMP(0, 3);
}

constexpr int PART_ROW = 144;
constexpr int PART_W = 128 * PART_ROW;
constexpr int K2_OBUF = 8 * PART_W;
constexpr int K2_DUMMY = K2_OBUF + 8 * D_ * 2;
constexpr int K2_SMEM = K2_DUMMY + 256;

DI float wave_sum(float v) {
  v += __builtin_bit_cast(float, __builtin_amdgcn_update_dpp(0, __builtin_bit_cast(int, v), 0x111, 0xf, 0xf, false));
  v += __builtin_bit_cast(float, __builtin_amdgcn_update_dpp(0, __builtin_bit_cast(int, v), 0x112, 0xf, 0xf, false));
  v += __builtin_bit_cast(float, __builtin_amdgcn_update_dpp(0, __builtin_bit_cast(int, v), 0x114, 0xf, 0xf, false));
  v += __builtin_bit_cast(float, __builtin_amdgcn_update_dpp(0, __builtin_bit_cast(int, v), 0x118, 0xf, 0xf, false));
  v += __builtin_bit_cast(float, __builtin_amdgcn_update_dpp(0, __builtin_bit_cast(int, v), 0x142, 0xa, 0xf, false));
  v += __builtin_bit_cast(float, __builtin_amdgcn_update_dpp(0, __builtin_bit_cast(int, v), 0x143, 0xc, 0xf, false));
  return __builtin_bit_cast(float, __builtin_amdgcn_readlane(__builtin_bit_cast(int, v), 63));
}

DI void attn_phase(char* smem, const u16* __restrict__ Q, const u16* __restrict__ Kf,
                   const u16* __restrict__ Vf, const u16* __restrict__ WoF, const u16* __restrict__ x,
                   u16* Hout, u16* HnF, const u16* __restrict__ w1, const u16* __restrict__ w2) {
  const int tid = threadIdx.x, l = tid & 63, h = l >> 5;
  const int w = __builtin_amdgcn_readfirstlane(tid >> 6);
  const int t0 = blockIdx.x * 8;

  STAMP(1, 0);
  if (w < 4) __builtin_amdgcn_s_setprio(2);
  const u32x4* kbase = (const u32x4*)Kf + (size_t)w * 8 * 128 + l;
  const u32x4* vbase = (const u32x4*)Vf + (size_t)w * 8 * 128 + l;
  u32x4 qf[4][2];
#pragma unroll
  for (int b = 0; b < 4; ++b)
#pragma unroll
    for (int s = 0; s < 2; ++s)
      qf[b][s] = *(const u32x4*)(Q + ((size_t)((blockIdx.x * 4 + b) * 2 + s) * 64 + l) * 8);

  const int role = (w + (blockIdx.x >> 3)) & 7;
  const int kh = role & 1, nq = role >> 1;
  const u32x4* wbase = (const u32x4*)WoF + (size_t)(role * 49) * 64 + l;
  constexpr int WO_EARLY = 0;
  u32x4 woe[WO_EARLY + 1];

  f32x16 zero16;
#pragma unroll
  for (int i = 0; i < 16; ++i) zero16[i] = 0.f;

  float mval[4];
  f32x16 o[4];
  int nmax = 1;
#pragma unroll
  for (int e = 0; e < WO_EARLY; ++e) woe[e] = wbase[e * 64];
  for (;;) {
    asm volatile("" : "+v"(kbase), "+v"(vbase));
    u32x4 k0n = kbase[0], k1n = kbase[64], v0n = vbase[0], v1n = vbase[64];
    u32x4 k0m = kbase[128], k1m = kbase[192], v0m = vbase[128], v1m = vbase[192];
    float mx[4];
#pragma unroll
    for (int b = 0; b < 4; ++b) {
      if (h) { qf[b][1][2] = 0u; qf[b][1][3] = 0u; }
      f32x16 s = MFMA32(asbf(k0n), asbf(qf[b][0]), zero16);
      s = MFMA32(asbf(k1n), asbf(qf[b][1]), s);
      float m = s[0];
#pragma unroll
      for (int i = 1; i < 16; ++i) m = fmaxf(m, s[i]);
      mx[b] = m;
    }
    for (int kb = 1; kb < nmax; ++kb) {
      const u32x4 k0 = kbase[kb * 128], k1 = kbase[kb * 128 + 64];
#pragma unroll
      for (int b = 0; b < 4; ++b) {
        f32x16 s = MFMA32(asbf(k0), asbf(qf[b][0]), zero16);
        s = MFMA32(asbf(k1), asbf(qf[b][1]), s);
#pragma unroll
        for (int i = 0; i < 16; ++i) mx[b] = fmaxf(mx[b], s[i]);
      }
    }
#pragma unroll
    for (int b = 0; b < 4; ++b) {
      const auto sw = __builtin_amdgcn_permlane32_swap(__float_as_uint(mx[b]), __float_as_uint(mx[b]), false, false);
      const float m = fmaxf(__uint_as_float(sw[0]), __uint_as_float(sw[1]));
      mval[b] = m;
      const float nm = -m;
      const unsigned hi = __float_as_uint(nm) & 0xffff0000u;
      const float r1 = nm - __uint_as_float(hi);
      const unsigned mid = __float_as_uint(r1) & 0xffff0000u;
      const float r2 = r1 - __uint_as_float(mid);
      const unsigned lo = __float_as_uint(r2) & 0xffff0000u;
      if (h) { qf[b][1][2] = (hi >> 16) | mid; qf[b][1][3] = lo >> 16; }
      o[b] = zero16;
    }
    STAMP(1, 1);
#define SB() __builtin_amdgcn_sched_barrier(0)
#define EXP4(S, i) do { S[i] = __builtin_amdgcn_exp2f(S[i]); S[i + 1] = __builtin_amdgcn_exp2f(S[i + 1]); \
                        S[i + 2] = __builtin_amdgcn_exp2f(S[i + 2]); S[i + 3] = __builtin_amdgcn_exp2f(S[i + 3]); } while (0)
#define STEP(SC, SN, B, BN, BP, KN0, KN1, V0, VP1) do { \
      u32x4 p0; \
      o[BP] = MFMA32(asbf(VP1), asbf(pp), o[BP]); SB(); \
      EXP4(SC, 0); SB(); \
      SN = MFMA32(asbf(KN0), asbf(qf[BN][0]), zero16); SB(); \
      EXP4(SC, 4); \
      p0[0] = pack2(SC[0], SC[1]); p0[1] = pack2(SC[2], SC[3]); p0[2] = pack2(SC[4], SC[5]); p0[3] = pack2(SC[6], SC[7]); SB(); \
      asm volatile("" :: "v"(pp)); \
      SN = MFMA32(asbf(KN1), asbf(qf[BN][1]), SN); SB(); \
      EXP4(SC, 8); SB(); \
      o[B] = MFMA32(asbf(V0), asbf(p0), o[B]); SB(); \
      EXP4(SC, 12); \
      pp[0] = pack2(SC[8], SC[9]); pp[1] = pack2(SC[10], SC[11]); pp[2] = pack2(SC[12], SC[13]); pp[3] = pack2(SC[14], SC[15]); SB(); \
      asm volatile("" :: "v"(p0)); \
    } while (0)
    {
      u32x4 kA0 = k0n, kA1 = k1n, vA0 = v0n, vA1 = v1n;
      u32x4 kB0 = k0m, kB1 = k1m, vB0 = v0m, vB1 = v1m;
      u32x4 kC0, kC1, vC0, vC1 = v1n;
      u32x4 pp = {0u, 0u, 0u, 0u};
      f32x16 sa, sb;
      sa = MFMA32(asbf(kA0), asbf(qf[0][0]), zero16);
      sa = MFMA32(asbf(kA1), asbf(qf[0][1]), sa);
#define BLOCK(KB, X, Y, Z, SA, SB_) do { \
        const int kn_ = ((KB) + 2) & 7; \
        k##Z##0 = kbase[kn_ * 128]; k##Z##1 = kbase[kn_ * 128 + 64]; v##Z##0 = vbase[kn_ * 128]; \
        SB(); \
        STEP(SA, SB_, 0, 1, 3, k##X##0, k##X##1, v##X##0, v##Z##1); \
        v##Z##1 = vbase[kn_ * 128 + 64]; \
        SB(); \
        STEP(SB_, SA, 1, 2, 0, k##X##0, k##X##1, v##X##0, v##X##1); \
        STEP(SA, SB_, 2, 3, 1, k##X##0, k##X##1, v##X##0, v##X##1); \
        STEP(SB_, SA, 3, 0, 2, k##Y##0, k##Y##1, v##X##0, v##X##1); \
      } while (0)
      auto pf_at = [&](int blk) {
        const bool j1 = blk == ((w * 3) & 7), j2 = (w == 1 && blk == 6);
        if (j1 || j2) {
          const int job = j1 ? w : 8;
          const int xcd = blockIdx.x & 7, bi = blockIdx.x >> 3;
          const u16* pf;
          if (job < 5) {
            const int idx = job * 64 + l, row = idx / 5, ln = idx - row * 5;
            int col = ((xcd * 49) >> 1) / 7 * 64 + ln * 64;
            col = col < F_ - 64 ? col : F_ - 64;
            pf = w1 + (size_t)(64 * bi + row) * F_ + col;
          } else {
            int idx = (job - 5) * 64 + l;
            idx = idx < 224 ? idx : 223;
            pf = w2 + (size_t)(56 * bi + (idx >> 2)) * S_ + 256 * xcd + (idx & 3) * 64;
          }
          touch(pf, (unsigned)(size_t)(smem + K2_DUMMY));
        }
      };
#pragma unroll 1
      for (int kb = 0; kb < 6; kb += 3) {
        BLOCK(kb, A, B, C, sa, sb);
        pf_at(kb);
        BLOCK(kb + 1, B, C, A, sa, sb);
        pf_at(kb + 1);
        BLOCK(kb + 2, C, A, B, sa, sb);
        pf_at(kb + 2);
      }
      BLOCK(6, A, B, C, sa, sb);
      pf_at(6);
      BLOCK(7, B, C, A, sa, sb);
      pf_at(7);
      o[3] = MFMA32(asbf(vB1), asbf(pp), o[3]);
    }
    bool ok = true;
#pragma unroll
    for (int b = 0; b < 4; ++b) ok = ok && (h == 0 || o[b][12] < 1.8446744e19f);
    if (nmax == 8 || __all(ok)) break;
    nmax = 8;
  }
  STAMP(1, 2);
  {
    char* pw = smem + w * PART_W;
#pragma unroll
    for (int b = 0; b < 4; ++b) {
      char* pr = pw + (32 * b + (l & 31)) * PART_ROW + 16 * h;
#pragma unroll
      for (int gq = 0; gq < 4; ++gq)
        *(f32x4*)(pr + 32 * gq) = (f32x4){o[b][4 * gq], o[b][4 * gq + 1], o[b][4 * gq + 2], o[b][4 * gq + 3]};
      if (h == 0) *(float*)(pr + 128) = mval[b];
    }
  }
  u32x4 xr;
  __builtin_amdgcn_s_setprio(0);
#define LOAD_WOF(S0, S1) do { \
    _Pragma("unroll") for (int s = S0; s < S1; ++s) \
      _Pragma("unroll") for (int i = 0; i < 7; ++i) \
        wof[s][i] = (s * 7 + i < WO_EARLY) ? woe[s * 7 + i] : wbase[(s * 7 + i) * 64]; \
  } while (0)
#define LOAD_XR() do { xr = *(const u32x4*)(x + (t0 + w) * D_ + 8 * (l < 56 ? l : 55)); } while (0)
#define WO_GEMM() do { \
    const int g = l >> 4; \
    f32x4 acc[7]; \
    _Pragma("unroll") for (int i = 0; i < 7; ++i) acc[i] = (f32x4){0.f, 0.f, 0.f, 0.f}; \
    _Pragma("unroll") for (int s = 0; s < 7; ++s) { \
      const u32x4 of = *(const u32x4*)(smem + K2_OBUF + (l & 7) * (D_ * 2) + 64 * (7 * kh + s) + 16 * g); \
      _Pragma("unroll") for (int i = 0; i < 7; ++i) acc[i] = MFMA16(asbf(wof[s][i]), asbf(of), acc[i]); \
    } \
    if ((l & 15) < 8) { \
      _Pragma("unroll") for (int i = 0; i < 7; ++i) \
        *(f32x4*)(smem + ((kh * 8 + (l & 15)) * D_ + 16 * (7 * nq + i) + 4 * g) * 4) = acc[i]; \
    } \
  } while (0)
  if (w < 4) {
    u32x4 wof[7][7];
    LOAD_WOF(0, 6);
    __builtin_amdgcn_sched_barrier(0);
    STAMP(1, 3);
    __syncthreads();
    STAMP(1, 4);
#pragma unroll 1
    for (int rep = 0; rep < 2; ++rep) {
      const int item = rep * 256 + tid;
      const int row = item >> 2, qd = item & 3;
      const char* pr0 = smem + row * PART_ROW;
      float M = -3.0e38f;
#pragma unroll
      for (int ww = 0; ww < 8; ++ww) M = fmaxf(M, *(const float*)(pr0 + ww * PART_W + 128));
      float lsum = 0.f;
      f32x4 a0 = {0.f, 0.f, 0.f, 0.f}, a1 = {0.f, 0.f, 0.f, 0.f};
#pragma unroll
      for (int ww = 0; ww < 8; ++ww) {
        const char* pr = pr0 + ww * PART_W;
        const float e = __builtin_amdgcn_exp2f(*(const float*)(pr + 128) - M);
        lsum += e * *(const float*)(pr + 112);
        a0 += e * *(const f32x4*)(pr + 32 * qd);
        a1 += e * *(const f32x4*)(pr + 32 * qd + 16);
      }
      const float inv = __builtin_amdgcn_rcpf(lsum);
      char* ob = smem + K2_OBUF + (row >> 4) * (D_ * 2) + (row & 15) * (DH_ * 2) + 16 * qd;
      u32x2 o0, o1;
      o0[0] = pack2(a0[0] * inv, a0[1] * inv); o0[1] = pack2(a0[2] * inv, a0[3] * inv);
      o1[0] = pack2(a1[0] * inv, a1[1] * inv); o1[1] = pack2(a1[2] * inv, a1[3] * inv);
      *(u32x2*)ob = o0;
      if (qd < 3) *(u32x2*)(ob + 8) = o1;
    }
    LOAD_WOF(6, 7);
    LOAD_XR();
    __builtin_amdgcn_sched_barrier(0);
    STAMP(1, 5);
    __syncthreads();
    STAMP(1, 6);
    WO_GEMM();
  } else {
    STAMP(1, 3);
    __syncthreads();
    STAMP(1, 4);
    u32x4 wof[7][7];
    LOAD_WOF(0, 7);
    LOAD_XR();
    __builtin_amdgcn_sched_barrier(0);
    STAMP(1, 5);
    __syncthreads();
    STAMP(1, 6);
    WO_GEMM();
  }
  STAMP(1, 7);
  __syncthreads();
  STAMP(1, 8);
  {
    const int lc = l < 56 ? l : 55;
    const float live = l < 56 ? 1.0f : 0.0f;
    const char* r0 = smem + (w * D_ + 8 * lc) * 4;
    const f32x4 a0 = *(const f32x4*)r0 + *(const f32x4*)(r0 + 8 * D_ * 4);
    const f32x4 a1 = *(const f32x4*)(r0 + 16) + *(const f32x4*)(r0 + 8 * D_ * 4 + 16);
    u32x4 hv;
    float v[8];
#pragma unroll
    for (int j = 0; j < 4; ++j) {
      const unsigned ab = j < 2 ? pack2(a0[2 * j], a0[2 * j + 1]) : pack2(a1[2 * j - 4], a1[2 * j - 3]);
      hv[j] = pack2(bflo(xr[j]) + bflo(ab), bfhi(xr[j]) + bfhi(ab));
      v[2 * j] = bflo(hv[j]); v[2 * j + 1] = bfhi(hv[j]);
    }
    if (l < 56) store_sc1(Hout + (t0 + w) * D_ + 8 * l, hv);
#pragma unroll
    for (int rep = 0; rep < 2; ++rep) {
      const float c = __builtin_bit_cast(float, __builtin_amdgcn_readfirstlane(__builtin_bit_cast(int, v[0])));
      float s1 = 0.f, s2 = 0.f;
#pragma unroll
      for (int i = 0; i < 8; ++i) { const float a = v[i] - c; s1 += a; s2 = fmaf(a, a, s2); }
      s1 = wave_sum(s1 * live) * (1.0f / D_);
      s2 = wave_sum(s2 * live) * (1.0f / D_);
      const float mean = c + s1;
      const float var = fmaxf(s2 - s1 * s1, 0.f);
      const float mb = bfr(mean);
      const float rs = __builtin_amdgcn_rcpf(bfr(__builtin_amdgcn_sqrtf(bfr(bfr(var) + 1e-5f))));
      const float nb = -mb * rs;
#pragma unroll
      for (int j = 0; j < 4; ++j) {
        hv[j] = pack2(fmaf(v[2 * j], rs, nb), fmaf(v[2 * j + 1], rs, nb));
        v[2 * j] = bflo(hv[j]); v[2 * j + 1] = bfhi(hv[j]);
      }
    }
    if (l < 56) *(u32x4*)(smem + K2_OBUF + (w * D_ + 8 * l) * 2) = hv;
  }
  STAMP(1, 9);
  __syncthreads();
  STAMP(1, 10);
  if (tid < D_) {
    const u16* hb = (const u16*)(smem + K2_OBUF);
    u32x4 v;
#pragma unroll
    for (int j = 0; j < 4; ++j)
      v[j] = (unsigned)hb[(2 * j) * D_ + tid] | ((unsigned)hb[(2 * j + 1) * D_ + tid] << 16);
    const int lane = ((t0 >> 3) & 3) * 16 + (tid & 15);
    store_sc1(HnF + ((size_t)((((tid >> 6) * 64 + (t0 >> 5)) * 4 + ((tid >> 4) & 3)) * 64 + lane)) * 8, v);
  }
  STAMP(1, 11);
}

constexpr int G_SMEM = 8 * 16384;
constexpr int GDEPTH = 4;

template <int MODE>
DI void gemm_phase(char* smem, int dt, int nt, const u16* __restrict__ W, const u16* act,
                   const u16* __restrict__ bias, const u16* Hres, u16* out,
                   unsigned* wait_cnt, int wait_n, unsigned wait_target) {
  constexpr int LDW = MODE == 0 ? F_ : S_;
  constexpr int NKS = MODE == 0 ? S_ / 32 : F_ / 32;
  constexpr int KPW = NKS / 8;
  const int tid = threadIdx.x, l = tid & 63;
  const int w = __builtin_amdgcn_readfirstlane(tid >> 6);
  char* ring = smem + w * (GDEPTH * 4096);
  const int g = l >> 4, i16 = l & 15, q4 = i16 >> 2, p4 = l & 3;
  const u16* asrc = act + ((size_t)dt * NKS + w * KPW) * 2048 + l * 8;
  const int fsrc = (l >> 4) & 1;
  const u16* wsrc = W + (size_t)(w * KPW * 32 + (l >> 3)) * LDW + 64 * nt + (l & 1) * 8;
  const int c0 = (l & 7) >> 1;
  const int ecol = tid >> 3, er8 = (tid & 7) * 8;
  const unsigned ring_a = (unsigned)(size_t)ring;

  u32x4 ar[GDEPTH][4];
  auto issueW = [&](int i) {
#pragma unroll
    for (int j = 0; j < 4; ++j) {
      const int c = c0 ^ (fsrc | ((j & 1) << 1));
      unsigned keep;
      asm volatile("s_mov_b32 %0, m0\n\ts_mov_b32 m0, %2\n\ts_nop 0\n\tglobal_load_lds_dwordx4 %1, off\n\ts_mov_b32 m0, %0"
                   : "=&s"(keep) : "v"(wsrc + (size_t)(i * 32 + 8 * j) * LDW + c * 16),
                     "s"(ring_a + (i % GDEPTH) * 4096 + j * 1024) : "memory");
    }
  };
  auto issueA = [&](int i) {
#pragma unroll
    for (int j = 0; j < 4; ++j)
      asm volatile("global_load_dwordx4 %0, %1, off sc1" : "=v"(ar[i % GDEPTH][j]) : "v"(asrc + (size_t)(i * 4 + j) * 512) : "memory");
  };

  const int fl = ((q4 >> 1) & 1) | ((g & 1) << 1);
  unsigned trb[4];
#pragma unroll
  for (int t = 0; t < 4; ++t) trb[t] = ring_a + (8 * g + q4) * 128 + p4 * 8 + ((t ^ fl) * 32);
#define G_TRREAD(DST, SL, N, ...) asm volatile( \
          "s_waitcnt vmcnt(%c10)\n\t" \
          "ds_read_b64_tr_b16 %0, %11 offset:%c8\n\t" \
          "ds_read_b64_tr_b16 %1, %11 offset:%c9\n\t" \
          "ds_read_b64_tr_b16 %2, %12 offset:%c8\n\t" \
          "ds_read_b64_tr_b16 %3, %12 offset:%c9\n\t" \
          "ds_read_b64_tr_b16 %4, %13 offset:%c8\n\t" \
          "ds_read_b64_tr_b16 %5, %13 offset:%c9\n\t" \
          "ds_read_b64_tr_b16 %6, %14 offset:%c8\n\t" \
          "ds_read_b64_tr_b16 %7, %14 offset:%c9\n\t" \
          "s_waitcnt lgkmcnt(0)" \
          : "=&v"(DST[0]), "=&v"(DST[1]), "=&v"(DST[2]), "=&v"(DST[3]), "=&v"(DST[4]), "=&v"(DST[5]), "=&v"(DST[6]), "=&v"(DST[7]) \
          : "i"((SL) * 4096), "i"((SL) * 4096 + 512), "i"(N), "v"(trb[0]), "v"(trb[1]), "v"(trb[2]), "v"(trb[3]) : "memory")
#define G_WF(R, t) asbf((u32x4){R[2 * (t)][0], R[2 * (t)][1], R[2 * (t) + 1][0], R[2 * (t) + 1][1]})

  STAMP(2 + MODE, 0);
  u32x2 wreg[GDEPTH][8];
#pragma unroll
  for (int i = 0; i < GDEPTH; ++i) issueW(i);
#pragma unroll
  for (int i = 0; i < GDEPTH; ++i) {
    G_TRREAD(wreg[i], i, 4 * (GDEPTH - 1 - i) + 4 * ((i + GDEPTH <= KPW ? i : KPW - GDEPTH)));
    if (i + GDEPTH < KPW) issueW(i + GDEPTH);
  }
  if (w == 0) wait_counters(wait_cnt, wait_n, wait_target, l);
  asm volatile("s_waitcnt lgkmcnt(0)" ::: "memory");
  __builtin_amdgcn_s_barrier();
  asm volatile("" ::: "memory");
  STAMP(2 + MODE, 1);
#pragma unroll
  for (int i = 0; i < GDEPTH; ++i) issueA(i);

  f32x4 acc[4][4];
#pragma unroll
  for (int a = 0; a < 4; ++a)
#pragma unroll
    for (int b = 0; b < 4; ++b) acc[a][b] = (f32x4){0.f, 0.f, 0.f, 0.f};

  u32x4 ep;
  unsigned epb = 0;
#pragma unroll
  for (int i = 0; i < KPW; ++i) {
    const int full = (KPW - 1 - i) < (GDEPTH - 1) ? (KPW - 1 - i) : (GDEPTH - 1);
    const int extra = (i == KPW - 1) ? (MODE == 0 ? 1 : 2) : 0;
    const int pend = full * 4 + extra;
    if (i == KPW - 1) {
      if (MODE == 0) {
        asm volatile("global_load_dwordx4 %0, %1, off" : "=v"(ep) : "v"(bias + 64 * nt + er8) : "memory");
      } else {
        asm volatile("global_load_dwordx4 %0, %1, off sc1" : "=v"(ep) : "v"(Hres + (size_t)(64 * nt + ecol) * D_ + 64 * dt + er8) : "memory");
        asm volatile("global_load_ushort %0, %1, off" : "=v"(epb) : "v"(bias + 64 * nt + ecol) : "memory");
      }
    }
    u32x4 a0 = ar[i % GDEPTH][0], a1 = ar[i % GDEPTH][1], a2 = ar[i % GDEPTH][2], a3 = ar[i % GDEPTH][3];
    u32x2 wl[8];
    if (i >= GDEPTH) G_TRREAD(wl, i - GDEPTH, 63);
    asm volatile("s_waitcnt vmcnt(%c4)" : "+v"(a0), "+v"(a1), "+v"(a2), "+v"(a3) : "i"(pend) : "memory");
    if (i + GDEPTH < KPW) issueA(i + GDEPTH);
    const bf16x8 af[4] = {asbf(a0), asbf(a1), asbf(a2), asbf(a3)};
    bf16x8 wf[4];
#pragma unroll
    for (int t = 0; t < 4; ++t) wf[t] = i < GDEPTH ? G_WF(wreg[i % GDEPTH], t) : G_WF(wl, t);
#pragma unroll
    for (int a = 0; a < 4; ++a)
#pragma unroll
      for (int b = 0; b < 4; ++b) {
        if (MODE == 0) acc[a][b] = MFMA16(wf[a], af[b], acc[a][b]);
        else           acc[a][b] = MFMA16(af[a], wf[b], acc[a][b]);
      }
  }
  STAMP(2 + MODE, 2);
  {
#pragma unroll
    for (int a = 0; a < 4; ++a)
#pragma unroll
      for (int b = 0; b < 4; ++b)
        *(f32x4*)(ring + (16 * b + i16) * 256 + (((4 * a + g) ^ i16) * 16)) = acc[a][b];
  }
  asm volatile("s_waitcnt vmcnt(0)" : "+v"(ep), "+v"(epb) :: "memory");
  STAMP(2 + MODE, 3);
  __syncthreads();
  {
    f32x4 s0 = {0.f, 0.f, 0.f, 0.f}, s1 = {0.f, 0.f, 0.f, 0.f};
    const int ch = (tid & 7) * 2;
    const char* p0 = smem + ecol * 256 + ((ch ^ (ecol & 15)) * 16);
    const char* p1 = smem + ecol * 256 + (((ch + 1) ^ (ecol & 15)) * 16);
#pragma unroll
    for (int ww = 0; ww < 8; ++ww) {
      s0 += *(const f32x4*)(p0 + ww * 16384);
      s1 += *(const f32x4*)(p1 + ww * 16384);
    }
    float v[8] = {s0[0], s0[1], s0[2], s0[3], s1[0], s1[1], s1[2], s1[3]};
    u32x4 ov;
    if (MODE == 0) {
      const int d = 64 * dt + ecol, f = 64 * nt + er8;
      constexpr float k1 = -2.0f * 0.7978845608028654f * 1.4426950408889634f;
      constexpr float k2 = k1 * 0.044715f;
#pragma unroll
      for (int j = 0; j < 4; ++j) {
        const unsigned tp = pack2(v[2 * j] + bflo(ep[j]), v[2 * j + 1] + bfhi(ep[j]));
        const float t0 = bflo(tp), t1 = bfhi(tp);
        const float e0 = __builtin_amdgcn_exp2f(t0 * fmaf(t0 * t0, k2, k1));
        const float e1 = __builtin_amdgcn_exp2f(t1 * fmaf(t1 * t1, k2, k1));
        ov[j] = pack2(t0 * __builtin_amdgcn_rcpf(1.0f + e0), t1 * __builtin_amdgcn_rcpf(1.0f + e1));
      }
      const int lane = ((f & 31) >> 3) * 16 + (d & 15);
      store_sc1(out + ((size_t)(((d >> 6) * (F_ / 32) + (f >> 5)) * 4 + ((d >> 4) & 3)) * 64 + lane) * 8, ov);
    } else {
      const int s = 64 * nt + ecol, d = 64 * dt + er8;
      const float bb = bf2f(epb);
#pragma unroll
      for (int j = 0; j < 4; ++j) {
        const unsigned yp = pack2(v[2 * j] + bb, v[2 * j + 1] + bb);
        ov[j] = pack2(bflo(ep[j]) + bflo(yp), bfhi(ep[j]) + bfhi(yp));
      }
      *(u32x4*)(out + (size_t)s * D_ + d) = ov;
    }
  }
  STAMP(2 + MODE, 4);
}

__global__ __launch_bounds__(512, 2) void k_fused(const u16* __restrict__ Q, const u16* __restrict__ Kf,
                                                  const u16* __restrict__ Vf, const u16* __restrict__ WoF,
                                                  const u16* __restrict__ x, u16* Hb, u16* HnF, u16* UF,
                                                  const u16* __restrict__ w1, const u16* __restrict__ b1,
                                                  const u16* __restrict__ w2, const u16* __restrict__ b2,
                                                  u16* out, unsigned* cnt) {
  extern __shared__ __attribute__((aligned(16))) char smem[];
  const int tid = threadIdx.x;
  const int xcd = blockIdx.x & 7, bi = blockIdx.x >> 3;
  unsigned* cntA = cnt;
  unsigned* cntB = cnt + NCNT_A * CNT_STRIDE;

  attn_phase(smem, Q, Kf, Vf, WoF, x, Hb, HnF, w1, w2);
  asm volatile("s_waitcnt vmcnt(0)" ::: "memory");
  __syncthreads();
  if (tid == 0) signal_counter(cntA + (blockIdx.x & (NCNT_A - 1)) * CNT_STRIDE);

  {
    const int start = (xcd * 49) >> 1, cntx = ((xcd + 1) * 49 >> 1) - start;
    if (bi < cntx) {
      const int j = start + bi, nt = j / 7, dt = j - nt * 7;
      gemm_phase<0>(smem, dt, nt, w1, HnF, b1, nullptr, UF, cntA, NCNT_A, 256 / NCNT_A);
      asm volatile("s_waitcnt vmcnt(0)" ::: "memory");
      __syncthreads();
      if (tid == 0) signal_counter(cntB + dt * CNT_STRIDE);
    }
  }
  if (bi < 28) {
    const int j = xcd * 28 + bi, nt = j / 7, dt = j - nt * 7;
    gemm_phase<1>(smem, dt, nt, w2, UF, b2, Hb, out, cntB + dt * CNT_STRIDE, 1, 28);
  }
}

extern "C" void kernel_launch(void* const* d_in, const int* in_sizes, int n_in, void* d_out, int out_size,
                              void* d_ws, size_t ws_size, hipStream_t stream) {
  const u16* x  = (const u16*)d_in[0];
  const u16* wq = (const u16*)d_in[1];
  const u16* wk = (const u16*)d_in[2];
  const u16* wv = (const u16*)d_in[3];
  const u16* wo = (const u16*)d_in[4];
  const u16* w1 = (const u16*)d_in[6];
  const u16* b1 = (const u16*)d_in[7];
  const u16* w2 = (const u16*)d_in[8];
  const u16* b2 = (const u16*)d_in[9];
  char* ws = (char*)d_ws;
  u16* Q   = (u16*)(ws + OFF_Q);
  u16* Kf  = (u16*)(ws + OFF_KF);
  u16* Vf  = (u16*)(ws + OFF_VF);
  u16* WoF = (u16*)(ws + OFF_WOF);
  u16* Hb  = (u16*)(ws + OFF_H);
  u16* HnF = (u16*)(ws + OFF_HNF);
  u16* UF  = (u16*)(ws + OFF_UF);

  unsigned* cnt = (unsigned*)(ws + OFF_CNT);

  (void)hipFuncSetAttribute((const void*)k_fused, hipFuncAttributeMaxDynamicSharedMemorySize, K2_SMEM);

  k1_ln_qkv<<<256, 256, 0, stream>>>(x, wq, wk, wv, wo, Q, Kf, Vf, WoF, cnt);
  k_fused<<<256, 512, K2_SMEM, stream>>>(Q, Kf, Vf, WoF, x, Hb, HnF, UF, w1, b1, w2, b2, (u16*)d_out, cnt);
}
```

```cpp
#include <hip/hip_runtime.h>
#include <hip/hip_bf16.h>
#include <stdint.h>

typedef unsigned short u16;
typedef __attribute__((ext_vector_type(8))) short bf16x8;
typedef __attribute__((ext_vector_type(4))) short s16x4;
typedef __attribute__((ext_vector_type(4))) float f32x4;
typedef __attribute__((ext_vector_type(16))) float f32x16;
typedef __attribute__((ext_vector_type(4))) unsigned u32x4;
typedef __attribute__((ext_vector_type(2))) unsigned u32x2;

#define DI __device__ __forceinline__
#define MFMA32(a, b, c) __builtin_amdgcn_mfma_f32_32x32x16_bf16((a), (b), (c), 0, 0, 0)
#define MFMA16(a, b, c) __builtin_amdgcn_mfma_f32_16x16x32_bf16((a), (b), (c), 0, 0, 0)

constexpr int S_ = 2048, D_ = 448, H_ = 16, DH_ = 28, F_ = 1792;

constexpr size_t OFF_Q   = 0;
constexpr size_t OFF_KF  = OFF_Q + (size_t)S_ * H_ * 32 * 2;
constexpr size_t OFF_VF  = OFF_KF + (size_t)S_ * 32 * 2;
constexpr size_t OFF_WOF = OFF_VF + (size_t)S_ * 32 * 2;
constexpr size_t OFF_H   = OFF_WOF + (size_t)D_ * D_ * 2;
constexpr size_t OFF_HNF = OFF_H + (size_t)S_ * D_ * 2;
constexpr size_t OFF_UF  = OFF_HNF + (size_t)S_ * D_ * 2;
constexpr size_t OFF_CNT = OFF_UF + (size_t)F_ * D_ * 2;

DI float bf2f(unsigned v) { return __uint_as_float(v << 16); }
DI float bflo(unsigned v) { return __uint_as_float(v << 16); }
DI float bfhi(unsigned v) { return __uint_as_float(v & 0xffff0000u); }
DI unsigned pack2(float a, float b) {
  typedef __attribute__((ext_vector_type(2))) __bf16 bf2;
  typedef __attribute__((ext_vector_type(2))) float f2;
  f2 v = {a, b};
  bf2 r = __builtin_convertvector(v, bf2);
  return __builtin_bit_cast(unsigned, r);
}
DI float bfr(float a) { return bflo(pack2(a, 0.f)); }
DI bf16x8 asbf(u32x4 v) { return __builtin_bit_cast(bf16x8, v); }

DI void glds16(const void* g, void* l) {
  __builtin_amdgcn_global_load_lds((const unsigned*)g, (unsigned*)l, 16, 0, 0);
}
DI void touch(const void* g, unsigned lds_dummy) {
  unsigned keep;
  asm volatile("s_mov_b32 %0, m0\n\ts_mov_b32 m0, %2\n\ts_nop 0\n\tglobal_load_lds_dword %1, off\n\ts_mov_b32 m0, %0"
               : "=&s"(keep) : "v"(g), "s"(lds_dummy) : "memory");
}
DI void store_sc1(void* g, u32x4 v) {
  asm volatile("global_store_dwordx4 %0, %1, off sc1\n\ts_nop 1" :: "v"(g), "v"(v) : "memory");
}
constexpr int NCNT_A = 16;
constexpr int CNT_STRIDE = 32;
DI void signal_counter(unsigned* c) {
  __hip_atomic_fetch_add(c, 1u, __ATOMIC_RELAXED, __HIP_MEMORY_SCOPE_AGENT);
}
DI void wait_counters(unsigned* c, int n, unsigned target, int l) {
  unsigned* p = c + (l < n ? l : 0) * CNT_STRIDE;
  for (int spin = 0; spin < 4000000; ++spin) {
    const unsigned v = __hip_atomic_load(p, __ATOMIC_RELAXED, __HIP_MEMORY_SCOPE_AGENT);
    if (__all(v >= target)) break;
    __builtin_amdgcn_s_sleep(1);
  }
}
#define STAMP(k, i) do {} while (0)

__global__ __launch_bounds__(256) void k1_ln_qkv(const u16* __restrict__ x, const u16* __restrict__ wq,
                                                 const u16* __restrict__ wk, const u16* __restrict__ wv,
                                                 const u16* __restrict__ wo, u16* __restrict__ Q,
                                                 u16* __restrict__ Kf, u16* __restrict__ Vf,
                                                 u16* __restrict__ WoF, unsigned* __restrict__ cnt) {
  __shared__ __attribute__((aligned(16))) char smem[2 * D_ * 128];
  const int tid = threadIdx.x, l = tid & 63;
  const int w = __builtin_amdgcn_readfirstlane(tid >> 6);
  const int bx = blockIdx.x, xcd = bx & 7, ii = bx >> 3;
  const int tb = xcd + 8 * (ii >> 3), nt = ii & 7;
  const int g = l >> 4, i16 = l & 15;

  STAMP(0, 0);
  if (bx == 0 && tid < NCNT_A + 7) cnt[tid * CNT_STRIDE] = 0u;
  unsigned wov[8];
  const bool do_wo = w < 2 && bx < 196;
  const int fi = do_wo ? 2 * bx + w : 0;
  {
    const u16* src = wo + (32 * (fi % 14) + 8 * g) * D_ + 16 * (fi / 14) + i16;
#pragma unroll
    for (int j = 0; j < 8; ++j)
      asm volatile("global_load_ushort %0, %1, off offset:%c2" : "=v"(wov[j]) : "v"(src + (j >> 2) * 4 * D_), "i"((j & 3) * D_ * 2) : "memory");
  }
  {
    const u16* xs = x + (size_t)(tb * 64 + w * 16) * D_;
    int row = l >= 56 ? 1 : 0, pos = l >= 56 ? l - 56 : l;
#pragma unroll
    for (int it = 0; it < 14; ++it) {
      glds16(xs + row * D_ + ((pos & ~7) | ((pos ^ row) & 7)) * 8, smem + D_ * 128 + w * 14336 + it * 1024);
      pos += 8; row += 1;
      if (pos >= 56) { pos -= 56; row += 1; }
    }
  }
  if (nt < 7) {
#pragma unroll
    for (int it = 0; it < 14; ++it) {
      const int piece = it * 4 + w;
      const int r = piece * 8 + (l >> 3);
      const int fr = ((r >> 1) & 1) | (((r >> 3) & 1) << 1);
      const int c = ((l & 7) >> 1) ^ fr;
      glds16(wq + r * D_ + nt * 64 + (2 * c + (l & 1)) * 8, smem + piece * 1024);
    }
  } else {
    const int col = (tid & 31) * 2, c16 = col & 31;
    const bool valid = c16 < DH_;
    const u16* wsrc = (col < 32 ? wk : wv) + (valid ? c16 : 0) + (tid >> 5) * DH_;
    unsigned v[56];
#pragma unroll
    for (int it = 0; it < 56; ++it) v[it] = *(const unsigned*)(wsrc + it * 8 * DH_);
#pragma unroll
    for (int it = 0; it < 56; ++it) {
      const int r = (tid >> 5) + 8 * it;
      const int fr = ((r >> 1) & 1) | (((r >> 3) & 1) << 1);
      *(unsigned*)(smem + r * 128 + (((col >> 4) ^ fr) * 32) + (col & 15) * 2) = valid ? v[it] : 0u;
    }
  }

  const int tok = tb * 64 + w * 16 + i16;
  STAMP(0, 1);
  asm volatile("s_waitcnt vmcnt(0)" : "+v"(wov[0]), "+v"(wov[1]), "+v"(wov[2]), "+v"(wov[3]), "+v"(wov[4]), "+v"(wov[5]),
               "+v"(wov[6]), "+v"(wov[7]) :: "memory");
  __syncthreads();
  STAMP(0, 2);
  u32x4 xf[14];
#pragma unroll
  for (int s = 0; s < 14; ++s) {
    const int c = 4 * s + g;
    xf[s] = *(const u32x4*)(smem + D_ * 128 + w * 14336 + i16 * 896 + ((c & ~7) | ((c ^ i16) & 7)) * 16);
  }
  const u32x4 ones = {0x3F803F80u, 0x3F803F80u, 0x3F803F80u, 0x3F803F80u};
  f32x4 accS = {0.f, 0.f, 0.f, 0.f}, accG = {0.f, 0.f, 0.f, 0.f};
  asm volatile("s_waitcnt lgkmcnt(0)" : "+v"(xf[0]), "+v"(xf[1]), "+v"(xf[2]), "+v"(xf[3]), "+v"(xf[4]), "+v"(xf[5]), "+v"(xf[6]),
               "+v"(xf[7]), "+v"(xf[8]), "+v"(xf[9]), "+v"(xf[10]), "+v"(xf[11]), "+v"(xf[12]), "+v"(xf[13]) :: "memory");

  const int q4 = i16 >> 2, p4 = l & 3;
  const int fl = ((q4 >> 1) & 1) | ((g & 1) << 1);
  unsigned trb[4];
#pragma unroll
  for (int t = 0; t < 4; ++t) trb[t] = (unsigned)(size_t)smem + (8 * g + q4) * 128 + p4 * 8 + ((t ^ fl) * 32);
  f32x4 acc[4], accC = {0.f, 0.f, 0.f, 0.f};
#pragma unroll
  for (int t = 0; t < 4; ++t) acc[t] = (f32x4){0.f, 0.f, 0.f, 0.f};
  u32x2 wr[3][8];
#define K1_RD7(S) asm volatile( \
          "ds_read_b64_tr_b16 %0, %7 offset:%c11\n\t" \
          "ds_read_b64_tr_b16 %1, %7 offset:%c11+512\n\t" \
          "ds_read_b64_tr_b16 %2, %8 offset:%c11\n\t" \
          "ds_read_b64_tr_b16 %3, %8 offset:%c11+512\n\t" \
          "ds_read_b64_tr_b16 %4, %9 offset:%c11\n\t" \
          "ds_read_b64_tr_b16 %5, %9 offset:%c11+512\n\t" \
          "ds_read_b64_tr_b16 %6, %10 offset:%c11" \
          : "=&v"(wr[(S) % 3][0]), "=&v"(wr[(S) % 3][1]), "=&v"(wr[(S) % 3][2]), "=&v"(wr[(S) % 3][3]), \
            "=&v"(wr[(S) % 3][4]), "=&v"(wr[(S) % 3][5]), "=&v"(wr[(S) % 3][6]) \
          : "v"(trb[0]), "v"(trb[1]), "v"(trb[2]), "v"(trb[3]), "i"((S) * 4096) : "memory")
#define K1_RD8(S) asm volatile("ds_read_b64_tr_b16 %0, %1 offset:%c2" : "=&v"(wr[(S) % 3][7]) : "v"(trb[3]), "i"((S) * 4096 + 512) : "memory")
#define K1_WAIT(S, N) asm volatile("s_waitcnt lgkmcnt(%c8)" \
          : "+v"(wr[(S) % 3][0]), "+v"(wr[(S) % 3][1]), "+v"(wr[(S) % 3][2]), "+v"(wr[(S) % 3][3]), \
            "+v"(wr[(S) % 3][4]), "+v"(wr[(S) % 3][5]), "+v"(wr[(S) % 3][6]), "+v"(wr[(S) % 3][7]) : "i"(N) : "memory")
#define K1_WF(S, t) asbf((u32x4){wr[(S) % 3][2 * (t)][0], wr[(S) % 3][2 * (t)][1], wr[(S) % 3][2 * (t) + 1][0], wr[(S) % 3][2 * (t) + 1][1]})
#define K1_LOOP(KV) do { \
    _Pragma("unroll") for (int s = 0; s < 14; ++s) { \
      if (s < 12) { K1_RD7(s + 2); K1_WAIT(s, 15); K1_RD8(s + 2); } \
      else if (s == 12) { K1_WAIT(s, 8); } \
      else { K1_WAIT(s, 0); } \
      accS = MFMA16(asbf(ones), asbf(xf[s]), accS);        \
      accG = MFMA16(asbf(xf[s]), asbf(xf[s]), accG);       \
      acc[0] = MFMA16(K1_WF(s, 0), asbf(xf[s]), acc[0]); \
      acc[1] = MFMA16(K1_WF(s, 1), asbf(xf[s]), acc[1]); \
      if (!(KV)) { \
        acc[2] = MFMA16(K1_WF(s, 2), asbf(xf[s]), acc[2]);        \
        acc[3] = MFMA16(K1_WF(s, 3), asbf(xf[s]), acc[3]); \
      } else { \
        acc[2] = MFMA16(asbf(xf[s]), K1_WF(s, 2), acc[2]);        \
        acc[3] = MFMA16(asbf(xf[s]), K1_WF(s, 3), acc[3]); \
      } \
    } } while (0)
  K1_RD7(0); K1_RD8(0); K1_RD7(1); K1_RD8(1);
  const bool kv = nt == 7;
  if (!kv) K1_LOOP(false); else K1_LOOP(true);
  {
    const unsigned cb = (unsigned)(size_t)smem + (8 * g + q4) * 128 + p4 * 8 + ((w ^ fl) * 32);
    u32x2 cr[14][2];
#pragma unroll
    for (int hs = 0; hs < 2; ++hs) {
      asm volatile(
          "ds_read_b64_tr_b16 %0, %14 offset:%c15\n\t"       "ds_read_b64_tr_b16 %1, %14 offset:%c15+512\n\t"
          "ds_read_b64_tr_b16 %2, %14 offset:%c15+4096\n\t"  "ds_read_b64_tr_b16 %3, %14 offset:%c15+4608\n\t"
          "ds_read_b64_tr_b16 %4, %14 offset:%c15+8192\n\t"  "ds_read_b64_tr_b16 %5, %14 offset:%c15+8704\n\t"
          "ds_read_b64_tr_b16 %6, %14 offset:%c15+12288\n\t" "ds_read_b64_tr_b16 %7, %14 offset:%c15+12800\n\t"
          "ds_read_b64_tr_b16 %8, %14 offset:%c15+16384\n\t" "ds_read_b64_tr_b16 %9, %14 offset:%c15+16896\n\t"
          "ds_read_b64_tr_b16 %10, %14 offset:%c15+20480\n\t" "ds_read_b64_tr_b16 %11, %14 offset:%c15+20992\n\t"
          "ds_read_b64_tr_b16 %12, %14 offset:%c15+24576\n\t" "ds_read_b64_tr_b16 %13, %14 offset:%c15+25088\n\t"
          "s_waitcnt lgkmcnt(0)"
          : "=&v"(cr[7 * hs][0]), "=&v"(cr[7 * hs][1]), "=&v"(cr[7 * hs + 1][0]), "=&v"(cr[7 * hs + 1][1]),
            "=&v"(cr[7 * hs + 2][0]), "=&v"(cr[7 * hs + 2][1]), "=&v"(cr[7 * hs + 3][0]), "=&v"(cr[7 * hs + 3][1]),
            "=&v"(cr[7 * hs + 4][0]), "=&v"(cr[7 * hs + 4][1]), "=&v"(cr[7 * hs + 5][0]), "=&v"(cr[7 * hs + 5][1]),
            "=&v"(cr[7 * hs + 6][0]), "=&v"(cr[7 * hs + 6][1])
          : "v"(cb), "i"(hs * 7 * 4096) : "memory");
    }
#pragma unroll
    for (int s = 0; s < 14; ++s)
      accC = MFMA16(asbf(ones), asbf((u32x4){cr[s][0][0], cr[s][0][1], cr[s][1][0], cr[s][1][1]}), accC);
  }
  __syncthreads();
  float* csb = (float*)smem;
  if (g == 0) csb[16 * w + i16] = accC[0];
  float mb, rs;
  {
    const int r3 = l & 3;
    const float cand = r3 == 0 ? accG[0] : r3 == 1 ? accG[1] : r3 == 2 ? accG[2] : accG[3];
    const float g2 = __shfl(cand, i16 + 16 * (i16 >> 2));
    const float mean = accS[0] * (1.0f / D_);
    const float var = fmaxf(g2 * (1.0f / D_) - mean * mean, 0.f);
    mb = bfr(mean);
    rs = __builtin_amdgcn_rcpf(bfr(__builtin_amdgcn_sqrtf(bfr(bfr(var) + 1e-5f))));
  }
  __syncthreads();
  if (!kv) {
    const float qs = rs * (0.18898223650461362f * 1.4426950408889634f);
#pragma unroll
    for (int t = 0; t < 4; ++t) {
      const int n = nt * 64 + 16 * t + 4 * g;
      const int head = n / DH_, d = n - head * DH_;
      const f32x4 cs = *(const f32x4*)(csb + 16 * t + 4 * g);
      u32x2 o;
      o[0] = pack2((acc[t][0] - mb * cs[0]) * qs, (acc[t][1] - mb * cs[1]) * qs);
      o[1] = pack2((acc[t][2] - mb * cs[2]) * qs, (acc[t][3] - mb * cs[3]) * qs);
      const int r = (tok & 7) * 16 + head;
      *(u32x2*)(Q + ((size_t)((((tok >> 3) * 4 + (r >> 5)) * 2 + (d >> 4)) * 64 + (r & 31) + 32 * ((d >> 3) & 1))) * 8 + (d & 7)) = o;
    }
  } else {
#pragma unroll
    for (int t = 0; t < 2; ++t) {
      const int d = 16 * t + 4 * g;
      const f32x4 cs = *(const f32x4*)(csb + d);
      u32x2 o;
      o[0] = pack2((acc[t][0] - mb * cs[0]) * rs, (acc[t][1] - mb * cs[1]) * rs);
      o[1] = pack2((acc[t][2] - mb * cs[2]) * rs, (acc[t][3] - mb * cs[3]) * rs);
      if (d == 28) { o[0] = 0x3F803F80u; o[1] = 0x00003F80u; }
      const int blk = tok >> 5, hh = (d & 15) >> 3, j = d & 7;
      *(u32x2*)(Kf + ((size_t)((blk * 2 + t) * 64 + (tok & 31) + 32 * hh)) * 8 + j) = o;
    }
    float mbk[4], rsk[4];
#pragma unroll
    for (int r = 0; r < 4; ++r) { mbk[r] = __shfl(mb, 4 * g + r); rsk[r] = __shfl(rs, 4 * g + r); }
#pragma unroll
    for (int t = 2; t < 4; ++t) {
      const int d = 16 * (t - 2) + i16;
      const float cs = csb[16 * t + i16];
      u32x2 o;
      o[0] = pack2((acc[t][0] - mbk[0] * cs) * rsk[0], (acc[t][1] - mbk[1] * cs) * rsk[1]);
      o[1] = pack2((acc[t][2] - mbk[2] * cs) * rsk[2], (acc[t][3] - mbk[3] * cs) * rsk[3]);
      if (d == 28) { o[0] = 0x3F803F80u; o[1] = 0x3F803F80u; }
      const int blk = tb * 2 + (w >> 1), s = w & 1;
      *(u32x2*)(Vf + ((size_t)((blk * 2 + s) * 64 + d + 32 * (g & 1))) * 8 + 4 * (g >> 1)) = o;
    }
  }
  if (do_wo) {
    const int t = fi / 14, s = fi % 14;
    u32x4 v;
#pragma unroll
    for (int j = 0; j < 4; ++j) v[j] = wov[2 * j] | (wov[2 * j + 1] << 16);
    const int dst = ((t / 7) * 2 + s / 7) * 49 + (s % 7) * 7 + t % 7;
    *(u32x4*)(WoF + ((size_t)dst * 64 + l) * 8) = v;
  }
  STAMP(0, 3);
}

constexpr int PART_ROW = 144;
constexpr int PART_W = 128 * PART_ROW;
constexpr int K2_OBUF = 8 * PART_W;
constexpr int K2_DUMMY = K2_OBUF + 8 * D_ * 2;
constexpr int K2_SMEM = K2_DUMMY + 256;

DI float wave_sum(float v) {
  v += __builtin_bit_cast(float, __builtin_amdgcn_update_dpp(0, __builtin_bit_cast(int, v), 0x111, 0xf, 0xf, false));
  v += __builtin_bit_cast(float, __builtin_amdgcn_update_dpp(0, __builtin_bit_cast(int, v), 0x112, 0xf, 0xf, false));
  v += __builtin_bit_cast(float, __builtin_amdgcn_update_dpp(0, __builtin_bit_cast(int, v), 0x114, 0xf, 0xf, false));
  v += __builtin_bit_cast(float, __builtin_amdgcn_update_dpp(0, __builtin_bit_cast(int, v), 0x118, 0xf, 0xf, false));
  v += __builtin_bit_cast(float, __builtin_amdgcn_update_dpp(0, __builtin_bit_cast(int, v), 0x142, 0xa, 0xf, false));
  v += __builtin_bit_cast(float, __builtin_amdgcn_update_dpp(0, __builtin_bit_cast(int, v), 0x143, 0xc, 0xf, false));
  return __builtin_bit_cast(float, __builtin_amdgcn_readlane(__builtin_bit_cast(int, v), 63));
}

DI void attn_phase(char* smem, const u16* __restrict__ Q, const u16* __restrict__ Kf,
                   const u16* __restrict__ Vf, const u16* __restrict__ WoF, const u16* __restrict__ x,
                   u16* Hout, u16* HnF, const u16* __restrict__ w1, const u16* __restrict__ w2) {
  const int tid = threadIdx.x, l = tid & 63, h = l >> 5;
  const int w = __builtin_amdgcn_readfirstlane(tid >> 6);
  const int t0 = blockIdx.x * 8;

  STAMP(1, 0);
  if (w < 4) __builtin_amdgcn_s_setprio(2);
  const u32x4* kbase = (const u32x4*)Kf + (size_t)w * 8 * 128 + l;
  const u32x4* vbase = (const u32x4*)Vf + (size_t)w * 8 * 128 + l;
  u32x4 qf[4][2];
#pragma unroll
  for (int b = 0; b < 4; ++b)
#pragma unroll
    for (int s = 0; s < 2; ++s)
      qf[b][s] = *(const u32x4*)(Q + ((size_t)((blockIdx.x * 4 + b) * 2 + s) * 64 + l) * 8);

  const int role = (w + (blockIdx.x >> 3)) & 7;
  const int kh = role & 1, nq = role >> 1;
  const u32x4* wbase = (const u32x4*)WoF + (size_t)(role * 49) * 64 + l;

  f32x16 zero16;
#pragma unroll
  for (int i = 0; i < 16; ++i) zero16[i] = 0.f;

  float mval[4];
  f32x16 o[4];
  int nmax = 1;
  for (;;) {
    asm volatile("" : "+v"(kbase), "+v"(vbase));
    u32x4 k0n = kbase[0], k1n = kbase[64], v0n = vbase[0], v1n = vbase[64];
    u32x4 k0m = kbase[128], k1m = kbase[192], v0m = vbase[128], v1m = vbase[192];
    float mx[4];
#pragma unroll
    for (int b = 0; b < 4; ++b) {
      if (h) { qf[b][1][2] = 0u; qf[b][1][3] = 0u; }
      f32x16 s = MFMA32(asbf(k0n), asbf(qf[b][0]), zero16);
      s = MFMA32(asbf(k1n), asbf(qf[b][1]), s);
      float m = s[0];
#pragma unroll
      for (int i = 1; i < 16; ++i) m = fmaxf(m, s[i]);
      mx[b] = m;
    }
    for (int kb = 1; kb < nmax; ++kb) {
      const u32x4 k0 = kbase[kb * 128], k1 = kbase[kb * 128 + 64];
#pragma unroll
      for (int b = 0; b < 4; ++b) {
        f32x16 s = MFMA32(asbf(k0), asbf(qf[b][0]), zero16);
        s = MFMA32(asbf(k1), asbf(qf[b][1]), s);
#pragma unroll
        for (int i = 0; i < 16; ++i) mx[b] = fmaxf(mx[b], s[i]);
      }
    }
#pragma unroll
    for (int b = 0; b < 4; ++b) {
      const auto sw = __builtin_amdgcn_permlane32_swap(__float_as_uint(mx[b]), __float_as_uint(mx[b]), false, false);
      const float m = fmaxf(__uint_as_float(sw[0]), __uint_as_float(sw[1]));
      mval[b] = m;
      const float nm = -m;
      const unsigned hi = __float_as_uint(nm) & 0xffff0000u;
      const float r1 = nm - __uint_as_float(hi);
      const unsigned mid = __float_as_uint(r1) & 0xffff0000u;
      const float r2 = r1 - __uint_as_float(mid);
      const unsigned lo = __float_as_uint(r2) & 0xffff0000u;
      if (h) { qf[b][1][2] = (hi >> 16) | mid; qf[b][1][3] = lo >> 16; }
      o[b] = zero16;
    }
    STAMP(1, 1);
#define SB() __builtin_amdgcn_sched_barrier(0)
#define EXP4(S, i) do { S[i] = __builtin_amdgcn_exp2f(S[i]); S[i + 1] = __builtin_amdgcn_exp2f(S[i + 1]); \
                        S[i + 2] = __builtin_amdgcn_exp2f(S[i + 2]); S[i + 3] = __builtin_amdgcn_exp2f(S[i + 3]); } while (0)
#define STEP(SC, SN, B, BN, BP, KN0, KN1, V0, VP1) do { \
      u32x4 p0; \
      o[BP] = MFMA32(asbf(VP1), asbf(pp), o[BP]); SB(); \
      EXP4(SC, 0); SB(); \
      SN = MFMA32(asbf(KN0), asbf(qf[BN][0]), zero16); SB(); \
      EXP4(SC, 4); \
      p0[0] = pack2(SC[0], SC[1]); p0[1] = pack2(SC[2], SC[3]); p0[2] = pack2(SC[4], SC[5]); p0[3] = pack2(SC[6], SC[7]); SB(); \
      asm volatile("" :: "v"(pp)); \
      SN = MFMA32(asbf(KN1), asbf(qf[BN][1]), SN); SB(); \
      EXP4(SC, 8); SB(); \
      o[B] = MFMA32(asbf(V0), asbf(p0), o[B]); SB(); \
      EXP4(SC, 12); \
      pp[0] = pack2(SC[8], SC[9]); pp[1] = pack2(SC[10], SC[11]); pp[2] = pack2(SC[12], SC[13]); pp[3] = pack2(SC[14], SC[15]); SB(); \
      asm volatile("" :: "v"(p0)); \
    } while (0)
    {
      u32x4 kA0 = k0n, kA1 = k1n, vA0 = v0n, vA1 = v1n;
      u32x4 kB0 = k0m, kB1 = k1m, vB0 = v0m, vB1 = v1m;
      u32x4 kC0, kC1, vC0, vC1 = v1n;
      u32x4 pp = {0u, 0u, 0u, 0u};
      f32x16 sa, sb;
      sa = MFMA32(asbf(kA0), asbf(qf[0][0]), zero16);
      sa = MFMA32(asbf(kA1), asbf(qf[0][1]), sa);
#define BLOCK(KB, X, Y, Z, SA, SB_) do { \
        const int kn_ = ((KB) + 2) & 7; \
        k##Z##0 = kbase[kn_ * 128]; k##Z##1 = kbase[kn_ * 128 + 64]; v##Z##0 = vbase[kn_ * 128]; \
        SB(); \
        STEP(SA, SB_, 0, 1, 3, k##X##0, k##X##1, v##X##0, v##Z##1); \
        v##Z##1 = vbase[kn_ * 128 + 64]; \
        SB(); \
        STEP(SB_, SA, 1, 2, 0, k##X##0, k##X##1, v##X##0, v##X##1); \
        STEP(SA, SB_, 2, 3, 1, k##X##0, k##X##1, v##X##0, v##X##1); \
        STEP(SB_, SA, 3, 0, 2, k##Y##0, k##Y##1, v##X##0, v##X##1); \
      } while (0)
      auto pf_at = [&](int blk) {
        const bool j1 = blk == ((w * 3) & 7), j2 = (w == 1 && blk == 6);
        if (j1 || j2) {
          const int job = j1 ? w : 8;
          const int xcd = blockIdx.x & 7, bi = blockIdx.x >> 3;
          const u16* pf;
          if (job < 5) {
            const int idx = job * 64 + l, row = idx / 5, ln = idx - row * 5;
            int col = ((xcd * 49) >> 1) / 7 * 64 + ln * 64;
            col = col < F_ - 64 ? col : F_ - 64;
            pf = w1 + (size_t)(64 * bi + row) * F_ + col;
          } else {
            int idx = (job - 5) * 64 + l;
            idx = idx < 224 ? idx : 223;
            pf = w2 + (size_t)(56 * bi + (idx >> 2)) * S_ + 256 * xcd + (idx & 3) * 64;
          }
          touch(pf, (unsigned)(size_t)(smem + K2_DUMMY));
        }
      };
#pragma unroll 1
      for (int kb = 0; kb < 6; kb += 3) {
        BLOCK(kb, A, B, C, sa, sb);
        pf_at(kb);
        BLOCK(kb + 1, B, C, A, sa, sb);
        pf_at(kb + 1);
        BLOCK(kb + 2, C, A, B, sa, sb);
        pf_at(kb + 2);
      }
      BLOCK(6, A, B, C, sa, sb);
      pf_at(6);
      BLOCK(7, B, C, A, sa, sb);
      pf_at(7);
      o[3] = MFMA32(asbf(vB1), asbf(pp), o[3]);
    }
    bool ok = true;
#pragma unroll
    for (int b = 0; b < 4; ++b) ok = ok && (h == 0 || o[b][12] < 1.8446744e19f);
    if (nmax == 8 || __all(ok)) break;
    nmax = 8;
  }
  STAMP(1, 2);
  {
    char* pw = smem + w * PART_W;
#pragma unroll
    for (int b = 0; b < 4; ++b) {
      char* pr = pw + (32 * b + (l & 31)) * PART_ROW + 16 * h;
#pragma unroll
      for (int gq = 0; gq < 4; ++gq)
        *(f32x4*)(pr + 32 * gq) = (f32x4){o[b][4 * gq], o[b][4 * gq + 1], o[b][4 * gq + 2], o[b][4 * gq + 3]};
      if (h == 0) *(float*)(pr + 128) = mval[b];
    }
  }
  u32x4 xr;
  __builtin_amdgcn_s_setprio(0);
#define LOAD_WOF(S0, S1) do { \
    _Pragma("unroll") for (int s = S0; s < S1; ++s) \
      _Pragma("unroll") for (int i = 0; i < 7; ++i) \
        wof[s][i] = wbase[(s * 7 + i) * 64]; \
  } while (0)
#define LOAD_XR() do { xr = *(const u32x4*)(x + (t0 + w) * D_ + 8 * (l < 56 ? l : 55)); } while (0)
#define WO_GEMM() do { \
    const int g = l >> 4; \
    f32x4 acc[7]; \
    _Pragma("unroll") for (int i = 0; i < 7; ++i) acc[i] = (f32x4){0.f, 0.f, 0.f, 0.f}; \
    _Pragma("unroll") for (int s = 0; s < 7; ++s) { \
      const u32x4 of = *(const u32x4*)(smem + K2_OBUF + (l & 7) * (D_ * 2) + 64 * (7 * kh + s) + 16 * g); \
      _Pragma("unroll") for (int i = 0; i < 7; ++i) acc[i] = MFMA16(asbf(wof[s][i]), asbf(of), acc[i]); \
    } \
    if ((l & 15) < 8) { \
      _Pragma("unroll") for (int i = 0; i < 7; ++i) \
        *(f32x4*)(smem + ((kh * 8 + (l & 15)) * D_ + 16 * (7 * nq + i) + 4 * g) * 4) = acc[i]; \
    } \
  } while (0)
  if (w < 4) {
    u32x4 wof[7][7];
    LOAD_WOF(0, 6);
    __builtin_amdgcn_sched_barrier(0);
    STAMP(1, 3);
    __syncthreads();
    STAMP(1, 4);
#pragma unroll 1
    for (int rep = 0; rep < 2; ++rep) {
      const int item = rep * 256 + tid;
      const int row = item >> 2, qd = item & 3;
      const char* pr0 = smem + row * PART_ROW;
      float M = -3.0e38f;
#pragma unroll
      for (int ww = 0; ww < 8; ++ww) M = fmaxf(M, *(const float*)(pr0 + ww * PART_W + 128));
      float lsum = 0.f;
      f32x4 a0 = {0.f, 0.f, 0.f, 0.f}, a1 = {0.f, 0.f, 0.f, 0.f};
#pragma unroll
      for (int ww = 0; ww < 8; ++ww) {
        const char* pr = pr0 + ww * PART_W;
        const float e = __builtin_amdgcn_exp2f(*(const float*)(pr + 128) - M);
        lsum += e * *(const float*)(pr + 112);
        a0 += e * *(const f32x4*)(pr + 32 * qd);
        a1 += e * *(const f32x4*)(pr + 32 * qd + 16);
      }
      const float inv = __builtin_amdgcn_rcpf(lsum);
      char* ob = smem + K2_OBUF + (row >> 4) * (D_ * 2) + (row & 15) * (DH_ * 2) + 16 * qd;
      u32x2 o0, o1;
      o0[0] = pack2(a0[0] * inv, a0[1] * inv); o0[1] = pack2(a0[2] * inv, a0[3] * inv);
      o1[0] = pack2(a1[0] * inv, a1[1] * inv); o1[1] = pack2(a1[2] * inv, a1[3] * inv);
      *(u32x2*)ob = o0;
      if (qd < 3) *(u32x2*)(ob + 8) = o1;
    }
    LOAD_WOF(6, 7);
    LOAD_XR();
    __builtin_amdgcn_sched_barrier(0);
    STAMP(1, 5);
    __syncthreads();
    STAMP(1, 6);
    WO_GEMM();
  } else {
    STAMP(1, 3);
    __syncthreads();
    STAMP(1, 4);
    u32x4 wof[7][7];
    LOAD_WOF(0, 7);
    LOAD_XR();
    __builtin_amdgcn_sched_barrier(0);
    STAMP(1, 5);
    __syncthreads();
    STAMP(1, 6);
    WO_GEMM();
  }
  STAMP(1, 7);
  __syncthreads();
  STAMP(1, 8);
  {
    const int lc = l < 56 ? l : 55;
    const float live = l < 56 ? 1.0f : 0.0f;
    const char* r0 = smem + (w * D_ + 8 * lc) * 4;
    const f32x4 a0 = *(const f32x4*)r0 + *(const f32x4*)(r0 + 8 * D_ * 4);
    const f32x4 a1 = *(const f32x4*)(r0 + 16) + *(const f32x4*)(r0 + 8 * D_ * 4 + 16);
    u32x4 hv;
    float v[8];
#pragma unroll
    for (int j = 0; j < 4; ++j) {
      const unsigned ab = j < 2 ? pack2(a0[2 * j], a0[2 * j + 1]) : pack2(a1[2 * j - 4], a1[2 * j - 3]);
      hv[j] = pack2(bflo(xr[j]) + bflo(ab), bfhi(xr[j]) + bfhi(ab));
      v[2 * j] = bflo(hv[j]); v[2 * j + 1] = bfhi(hv[j]);
    }
    if (l < 56) store_sc1(Hout + (t0 + w) * D_ + 8 * l, hv);
#pragma unroll
    for (int rep = 0; rep < 2; ++rep) {
      const float c = __builtin_bit_cast(float, __builtin_amdgcn_readfirstlane(__builtin_bit_cast(int, v[0])));
      float s1 = 0.f, s2 = 0.f;
#pragma unroll
      for (int i = 0; i < 8; ++i) { const float a = v[i] - c; s1 += a; s2 = fmaf(a, a, s2); }
      s1 = wave_sum(s1 * live) * (1.0f / D_);
      s2 = wave_sum(s2 * live) * (1.0f / D_);
      const float mean = c + s1;
      const float var = fmaxf(s2 - s1 * s1, 0.f);
      const float mb = bfr(mean);
      const float rs = __builtin_amdgcn_rcpf(bfr(__builtin_amdgcn_sqrtf(bfr(bfr(var) + 1e-5f))));
      const float nb = -mb * rs;
#pragma unroll
      for (int j = 0; j < 4; ++j) {
        hv[j] = pack2(fmaf(v[2 * j], rs, nb), fmaf(v[2 * j + 1], rs, nb));
        v[2 * j] = bflo(hv[j]); v[2 * j + 1] = bfhi(hv[j]);
      }
    }
    if (l < 56) *(u32x4*)(smem + K2_OBUF + (w * D_ + 8 * l) * 2) = hv;
  }
  STAMP(1, 9);
  __syncthreads();
  STAMP(1, 10);
  if (tid < D_) {
    const u16* hb = (const u16*)(smem + K2_OBUF);
    u32x4 v;
#pragma unroll
    for (int j = 0; j < 4; ++j)
      v[j] = (unsigned)hb[(2 * j) * D_ + tid] | ((unsigned)hb[(2 * j + 1) * D_ + tid] << 16);
    const int lane = ((t0 >> 3) & 3) * 16 + (tid & 15);
    store_sc1(HnF + ((size_t)((((tid >> 6) * 64 + (t0 >> 5)) * 4 + ((tid >> 4) & 3)) * 64 + lane)) * 8, v);
  }
  STAMP(1, 11);
}

constexpr int G_SMEM = 8 * 16384;
constexpr int GDEPTH = 4;

template <int MODE>
DI void gemm_phase(char* smem, int dt, int nt, const u16* __restrict__ W, const u16* act,
                   const u16* __restrict__ bias, const u16* Hres, u16* out,
                   unsigned* wait_cnt, int wait_n, unsigned wait_target) {
  constexpr int LDW = MODE == 0 ? F_ : S_;
  constexpr int NKS = MODE == 0 ? S_ / 32 : F_ / 32;
  constexpr int KPW = NKS / 8;
  const int tid = threadIdx.x, l = tid & 63;
  const int w = __builtin_amdgcn_readfirstlane(tid >> 6);
  char* ring = smem + w * (GDEPTH * 4096);
  const int g = l >> 4, i16 = l & 15, q4 = i16 >> 2, p4 = l & 3;
  const u16* asrc = act + ((size_t)dt * NKS + w * KPW) * 2048 + l * 8;
  const int fsrc = (l >> 4) & 1;
  const u16* wsrc = W + (size_t)(w * KPW * 32 + (l >> 3)) * LDW + 64 * nt + (l & 1) * 8;
  const int c0 = (l & 7) >> 1;
  const int ecol = tid >> 3, er8 = (tid & 7) * 8;
  const unsigned ring_a = (unsigned)(size_t)ring;

  u32x4 ar[GDEPTH][4];
  auto issueW = [&](int i) {
#pragma unroll
    for (int j = 0; j < 4; ++j) {
      const int c = c0 ^ (fsrc | ((j & 1) << 1));
      unsigned keep;
      asm volatile("s_mov_b32 %0, m0\n\ts_mov_b32 m0, %2\n\ts_nop 0\n\tglobal_load_lds_dwordx4 %1, off\n\ts_mov_b32 m0, %0"
                   : "=&s"(keep) : "v"(wsrc + (size_t)(i * 32 + 8 * j) * LDW + c * 16),
                     "s"(ring_a + (i % GDEPTH) * 4096 + j * 1024) : "memory");
    }
  };
  auto issueA = [&](int i) {
#pragma unroll
    for (int j = 0; j < 4; ++j)
      asm volatile("global_load_dwordx4 %0, %1, off sc1" : "=v"(ar[i % GDEPTH][j]) : "v"(asrc + (size_t)(i * 4 + j) * 512) : "memory");
  };

  const int fl = ((q4 >> 1) & 1) | ((g & 1) << 1);
  unsigned trb[4];
#pragma unroll
  for (int t = 0; t < 4; ++t) trb[t] = ring_a + (8 * g + q4) * 128 + p4 * 8 + ((t ^ fl) * 32);
#define G_TRREAD(DST, SL, N, ...) asm volatile( \
          "s_waitcnt vmcnt(%c10)\n\t" \
          "ds_read_b64_tr_b16 %0, %11 offset:%c8\n\t" \
          "ds_read_b64_tr_b16 %1, %11 offset:%c9\n\t" \
          "ds_read_b64_tr_b16 %2, %12 offset:%c8\n\t" \
          "ds_read_b64_tr_b16 %3, %12 offset:%c9\n\t" \
          "ds_read_b64_tr_b16 %4, %13 offset:%c8\n\t" \
          "ds_read_b64_tr_b16 %5, %13 offset:%c9\n\t" \
          "ds_read_b64_tr_b16 %6, %14 offset:%c8\n\t" \
          "ds_read_b64_tr_b16 %7, %14 offset:%c9\n\t" \
          "s_waitcnt lgkmcnt(0)" \
          : "=&v"(DST[0]), "=&v"(DST[1]), "=&v"(DST[2]), "=&v"(DST[3]), "=&v"(DST[4]), "=&v"(DST[5]), "=&v"(DST[6]), "=&v"(DST[7]) \
          : "i"((SL) * 4096), "i"((SL) * 4096 + 512), "i"(N), "v"(trb[0]), "v"(trb[1]), "v"(trb[2]), "v"(trb[3]) : "memory")
#define G_WF(R, t) asbf((u32x4){R[2 * (t)][0], R[2 * (t)][1], R[2 * (t) + 1][0], R[2 * (t) + 1][1]})

  STAMP(2 + MODE, 0);
  u32x2 wreg[GDEPTH][8];
#pragma unroll
  for (int i = 0; i < GDEPTH; ++i) issueW(i);
#pragma unroll
  for (int i = 0; i < GDEPTH; ++i) {
    G_TRREAD(wreg[i], i, 4 * (GDEPTH - 1 - i) + 4 * ((i + GDEPTH <= KPW ? i : KPW - GDEPTH)));
    if (i + GDEPTH < KPW) issueW(i + GDEPTH);
  }
  if (w == 0) wait_counters(wait_cnt, wait_n, wait_target, l);
  asm volatile("s_waitcnt lgkmcnt(0)" ::: "memory");
  __builtin_amdgcn_s_barrier();
  asm volatile("" ::: "memory");
  STAMP(2 + MODE, 1);
#pragma unroll
  for (int i = 0; i < GDEPTH; ++i) issueA(i);

  f32x4 acc[4][4];
#pragma unroll
  for (int a = 0; a < 4; ++a)
#pragma unroll
    for (int b = 0; b < 4; ++b) acc[a][b] = (f32x4){0.f, 0.f, 0.f, 0.f};

  u32x4 ep;
  unsigned epb = 0;
#pragma unroll
  for (int i = 0; i < KPW; ++i) {
    const int full = (KPW - 1 - i) < (GDEPTH - 1) ? (KPW - 1 - i) : (GDEPTH - 1);
    const int extra = (i == KPW - 1) ? (MODE == 0 ? 1 : 2) : 0;
    const int pend = full * 4 + extra;
    if (i == KPW - 1) {
      if (MODE == 0) {
        asm volatile("global_load_dwordx4 %0, %1, off" : "=v"(ep) : "v"(bias + 64 * nt + er8) : "memory");
      } else {
        asm volatile("global_load_dwordx4 %0, %1, off sc1" : "=v"(ep) : "v"(Hres + (size_t)(64 * nt + ecol) * D_ + 64 * dt + er8) : "memory");
        asm volatile("global_load_ushort %0, %1, off" : "=v"(epb) : "v"(bias + 64 * nt + ecol) : "memory");
      }
    }
    u32x4 a0 = ar[i % GDEPTH][0], a1 = ar[i % GDEPTH][1], a2 = ar[i % GDEPTH][2], a3 = ar[i % GDEPTH][3];
    u32x2 wl[8];
    if (i >= GDEPTH) G_TRREAD(wl, i - GDEPTH, 63);
    asm volatile("s_waitcnt vmcnt(%c4)" : "+v"(a0), "+v"(a1), "+v"(a2), "+v"(a3) : "i"(pend) : "memory");
    if (i + GDEPTH < KPW) issueA(i + GDEPTH);
    const bf16x8 af[4] = {asbf(a0), asbf(a1), asbf(a2), asbf(a3)};
    bf16x8 wf[4];
#pragma unroll
    for (int t = 0; t < 4; ++t) wf[t] = i < GDEPTH ? G_WF(wreg[i % GDEPTH], t) : G_WF(wl, t);
#pragma unroll
    for (int a = 0; a < 4; ++a)
#pragma unroll
      for (int b = 0; b < 4; ++b) {
        if (MODE == 0) acc[a][b] = MFMA16(wf[a], af[b], acc[a][b]);
        else           acc[a][b] = MFMA16(af[a], wf[b], acc[a][b]);
      }
  }
  STAMP(2 + MODE, 2);
  {
#pragma unroll
    for (int a = 0; a < 4; ++a)
#pragma unroll
      for (int b = 0; b < 4; ++b)
        *(f32x4*)(ring + (16 * b + i16) * 256 + (((4 * a + g) ^ i16) * 16)) = acc[a][b];
  }
  asm volatile("s_waitcnt vmcnt(0)" : "+v"(ep), "+v"(epb) :: "memory");
  STAMP(2 + MODE, 3);
  __syncthreads();
  {
    f32x4 s0 = {0.f, 0.f, 0.f, 0.f}, s1 = {0.f, 0.f, 0.f, 0.f};
    const int ch = (tid & 7) * 2;
    const char* p0 = smem + ecol * 256 + ((ch ^ (ecol & 15)) * 16);
    const char* p1 = smem + ecol * 256 + (((ch + 1) ^ (ecol & 15)) * 16);
#pragma unroll
    for (int ww = 0; ww < 8; ++ww) {
      s0 += *(const f32x4*)(p0 + ww * 16384);
      s1 += *(const f32x4*)(p1 + ww * 16384);
    }
    float v[8] = {s0[0], s0[1], s0[2], s0[3], s1[0], s1[1], s1[2], s1[3]};
    u32x4 ov;
    if (MODE == 0) {
      const int d = 64 * dt + ecol, f = 64 * nt + er8;
      constexpr float k1 = -2.0f * 0.7978845608028654f * 1.4426950408889634f;
      constexpr float k2 = k1 * 0.044715f;
#pragma unroll
      for (int j = 0; j < 4; ++j) {
        const unsigned tp = pack2(v[2 * j] + bflo(ep[j]), v[2 * j + 1] + bfhi(ep[j]));
        const float t0 = bflo(tp), t1 = bfhi(tp);
        const float e0 = __builtin_amdgcn_exp2f(t0 * fmaf(t0 * t0, k2, k1));
        const float e1 = __builtin_amdgcn_exp2f(t1 * fmaf(t1 * t1, k2, k1));
        ov[j] = pack2(t0 * __builtin_amdgcn_rcpf(1.0f + e0), t1 * __builtin_amdgcn_rcpf(1.0f + e1));
      }
      const int lane = ((f & 31) >> 3) * 16 + (d & 15);
      store_sc1(out + ((size_t)(((d >> 6) * (F_ / 32) + (f >> 5)) * 4 + ((d >> 4) & 3)) * 64 + lane) * 8, ov);
    } else {
      const int s = 64 * nt + ecol, d = 64 * dt + er8;
      const float bb = bf2f(epb);
#pragma unroll
      for (int j = 0; j < 4; ++j) {
        const unsigned yp = pack2(v[2 * j] + bb, v[2 * j + 1] + bb);
        ov[j] = pack2(bflo(ep[j]) + bflo(yp), bfhi(ep[j]) + bfhi(yp));
      }
      *(u32x4*)(out + (size_t)s * D_ + d) = ov;
    }
  }
  STAMP(2 + MODE, 4);
}

__global__ __launch_bounds__(512, 2) void k_fused(const u16* __restrict__ Q, const u16* __restrict__ Kf,
                                                  const u16* __restrict__ Vf, const u16* __restrict__ WoF,
                                                  const u16* __restrict__ x, u16* Hb, u16* HnF, u16* UF,
                                                  const u16* __restrict__ w1, const u16* __restrict__ b1,
                                                  const u16* __restrict__ w2, const u16* __restrict__ b2,
                                                  u16* out, unsigned* cnt) {
  extern __shared__ __attribute__((aligned(16))) char smem[];
  const int tid = threadIdx.x;
  const int xcd = blockIdx.x & 7, bi = blockIdx.x >> 3;
  unsigned* cntA = cnt;
  unsigned* cntB = cnt + NCNT_A * CNT_STRIDE;

  attn_phase(smem, Q, Kf, Vf, WoF, x, Hb, HnF, w1, w2);
  asm volatile("s_waitcnt vmcnt(0)" ::: "memory");
  __syncthreads();
  if (tid == 0) signal_counter(cntA + (blockIdx.x & (NCNT_A - 1)) * CNT_STRIDE);

  {
    const int start = (xcd * 49) >> 1, cntx = ((xcd + 1) * 49 >> 1) - start;
    if (bi < cntx) {
      const int j = start + bi, nt = j / 7, dt = j - nt * 7;
      gemm_phase<0>(smem, dt, nt, w1, HnF, b1, nullptr, UF, cntA, NCNT_A, 256 / NCNT_A);
      asm volatile("s_waitcnt vmcnt(0)" ::: "memory");
      __syncthreads();
      if (tid == 0) signal_counter(cntB + dt * CNT_STRIDE);
    }
  }
  if (bi < 28) {
    const int j = xcd * 28 + bi, nt = j / 7, dt = j - nt * 7;
    gemm_phase<1>(smem, dt, nt, w2, UF, b2, Hb, out, cntB + dt * CNT_STRIDE, 1, 28);
  }
}

extern "C" void kernel_launch(void* const* d_in, const int* in_sizes, int n_in, void* d_out, int out_size,
                              void* d_ws, size_t ws_size, hipStream_t stream) {
  const u16* x  = (const u16*)d_in[0];
  const u16* wq = (const u16*)d_in[1];
  const u16* wk = (const u16*)d_in[2];
  const u16* wv = (const u16*)d_in[3];
  const u16* wo = (const u16*)d_in[4];
  const u16* w1 = (const u16*)d_in[6];
  const u16* b1 = (const u16*)d_in[7];
  const u16* w2 = (const u16*)d_in[8];
  const u16* b2 = (const u16*)d_in[9];
  char* ws = (char*)d_ws;
  u16* Q   = (u16*)(ws + OFF_Q);
  u16* Kf  = (u16*)(ws + OFF_KF);
  u16* Vf  = (u16*)(ws + OFF_VF);
  u16* WoF = (u16*)(ws + OFF_WOF);
  u16* Hb  = (u16*)(ws + OFF_H);
  u16* HnF = (u16*)(ws + OFF_HNF);
  u16* UF  = (u16*)(ws + OFF_UF);

  unsigned* cnt = (unsigned*)(ws + OFF_CNT);

  (void)hipFuncSetAttribute((const void*)k_fused, hipFuncAttributeMaxDynamicSharedMemorySize, K2_SMEM);

  k1_ln_qkv<<<256, 256, 0, stream>>>(x, wq, wk, wv, wo, Q, Kf, Vf, WoF, cnt);
  k_fused<<<256, 512, K2_SMEM, stream>>>(Q, Kf, Vf, WoF, x, Hb, HnF, UF, w1, b1, w2, b2, (u16*)d_out, cnt);
}
```
